```python
import torch

def _softmax(x, axis=-1):
    return torch.softmax(x, dim=axis)
def _gelu(x):
    return torch.nn.functional.gelu(x, approximate="tanh")
def _silu(x):
    return torch.nn.functional.silu(x)
def _sigmoid(x):
    return torch.sigmoid(x)
def _layernorm(x, axis=-1, eps=1e-5):
    m = x.mean(dim=axis, keepdim=True)
    v = x.var(dim=axis, keepdim=True, unbiased=False)
    return (x - m) / torch.sqrt(v + eps)
def _scatter_add(tgt, idx, src, axis=0):
    return tgt.clone().index_add_(axis, idx, src)
def _segment_sum(src, seg, n):
    out = torch.zeros((n,) + src.shape[1:], dtype=src.dtype)
    return out.index_add_(0, seg, src)
def _pad(x, pw):
    flat = tuple(v for pair in reversed(pw) for v in pair)
    return torch.nn.functional.pad(x, flat)

kernel_name = 'recurrent_scan__spectral_gfnet__glu_gated_geglu__prompt'

def setup_inputs():

    _w = (lambda a: (lambda t: t.to(torch.bfloat16) if t.is_floating_point() else t)(torch.as_tensor(a)))
    def _A_setup():
        B, S, D = 1, 2048, 512
        Df = D // 2 + 1
        return {
            "x": _w((lambda *s: torch.randn(*s, dtype=torch.bfloat16) * 0.1)(B, S, D)),
            "w_r": _w((lambda *s: torch.randn(*s, dtype=torch.bfloat16) * 3.2)(S, Df) * 0.02),
            "w_i": _w((lambda *s: torch.randn(*s, dtype=torch.bfloat16) * 3.2)(S, Df) * 0.02),
        }
    _A_in = _A_setup()

    def _B_setup():
        B, S, Dm, Df = 1, 2048, 512, 1376
        return {
            "x": _w((lambda *s: torch.randn(*s, dtype=torch.bfloat16) * 0.1)(B, S, Dm)),
            "w_gate": _w((lambda *s: torch.randn(*s, dtype=torch.bfloat16) * 0.8)(Dm, Df) * (Dm ** -0.5)),
            "w_up": _w((lambda *s: torch.randn(*s, dtype=torch.bfloat16) * 0.8)(Dm, Df) * (Dm ** -0.5)),
            "w_down": _w((lambda *s: torch.randn(*s, dtype=torch.bfloat16) * 0.8)(Df, Dm) * (Df ** -0.5)),
        }
    _B_in = _B_setup()

    return {
        "A_x": _A_in["x"],
        "A_w_r": _A_in["w_r"],
        "A_w_i": _A_in["w_i"],
        "B_x": _B_in["x"],
        "B_w_gate": _B_in["w_gate"],
        "B_w_up": _B_in["w_up"],
        "B_w_down": _B_in["w_down"],
    }

def reference(A_x, A_w_r, A_w_i, B_x, B_w_gate, B_w_up, B_w_down):
    def _A_ref(x, w_r, w_i):
        B, S, D = x.shape
        X = (lambda x, axes=(-2,-1): torch.fft.rfft2(x.float(), dim=axes))(x, axes=(-2, -1))
        W = w_r + 1j * w_i
        X = X * W
        y = (lambda x, s, axes=(-2,-1): torch.fft.irfft2(x, s=s, dim=axes).to(torch.bfloat16))(X, (S, D), axes=(-2, -1))
        return y

    def _B_ref(x, w_gate, w_up, w_down):
        g = torch.einsum("bsd,df->bsf", x, w_gate)
        u = torch.einsum("bsd,df->bsf", x, w_up)
        h = _gelu(g) * u
        return torch.einsum("bsf,fd->bsd", h, w_down)

    x = A_x
    xn = _layernorm(x, axis=-1)
    s = _A_ref(xn, A_w_r, A_w_i)
    g = _silu(_B_ref(xn, B_w_gate, B_w_up, B_w_down))
    return x + s * g
```

```cpp
#include <hip/hip_runtime.h>
#include <hip/hip_bf16.h>
#include <stdint.h>

#define S_ 2048
#define D_ 512
#define F_ 1376
#define DF_ 257

__device__ __forceinline__ float bf2f(uint16_t v){ return __uint_as_float(((uint32_t)v)<<16); }
__device__ __forceinline__ uint16_t f2bf(float f){ uint32_t u=__float_as_uint(f); u += 0x7fffu + ((u>>16)&1u); return (uint16_t)(u>>16); }
__device__ __forceinline__ float rbf(float f){ return bf2f(f2bf(f)); }

__global__ void ln_k(const uint16_t* x, uint16_t* xn){
  int row=blockIdx.x; int t=threadIdx.x;
  float v[8]; float s=0;
  for(int i=0;i<8;i++){ v[i]=bf2f(x[row*D_+t*8+i]); s+=v[i]; }
  for(int m=32;m>=1;m>>=1) s+=__shfl_xor(s,m);
  float mean=s/D_;
  float q=0; for(int i=0;i<8;i++){ float d=v[i]-mean; q+=d*d; }
  for(int m=32;m>=1;m>>=1) q+=__shfl_xor(q,m);
  float var=rbf(q/D_);
  float mb=rbf(mean);
  float den=rbf(sqrtf(rbf(var+1e-5f)));
  for(int i=0;i<8;i++){ float d=rbf(v[i]-mb); xn[row*D_+t*8+i]=f2bf(d/den); }
}
__global__ void rowdft_k(const uint16_t* xn, float2* X){
  int row=blockIdx.x; int k=threadIdx.x; __shared__ float xs[D_];
  for(int i=threadIdx.x;i<D_;i+=blockDim.x) xs[i]=bf2f(xn[row*D_+i]);
  __syncthreads();
  if(k>=DF_) return;
  float re=0,im=0;
  for(int n=0;n<D_;n++){ int p=(n*k)&(D_-1); float sn,cs; sincospif(-2.0f*p/D_,&sn,&cs); re+=xs[n]*cs; im+=xs[n]*sn; }
  X[row*DF_+k]=make_float2(re,im);
}
__global__ void coldft_k(const float2* X, float2* Y, const uint16_t* wr, const uint16_t* wi, float sign){
  int k2=blockIdx.x; __shared__ float2 xs[S_]; __shared__ float2 tw[S_];
  for(int i=threadIdx.x;i<S_;i+=blockDim.x){ xs[i]=X[i*DF_+k2]; float sn,cs; sincospif(sign*2.0f*i/S_,&sn,&cs); tw[i]=make_float2(cs,sn);}
  __syncthreads();
  for(int k1=threadIdx.x;k1<S_;k1+=blockDim.x){
    float re=0,im=0;
    for(int n=0;n<S_;n++){ float2 w=tw[(n*k1)&(S_-1)]; float2 a=xs[n]; re+=a.x*w.x-a.y*w.y; im+=a.x*w.y+a.y*w.x; }
    if(wr){ float a=bf2f(wr[k1*DF_+k2]), b=bf2f(wi[k1*DF_+k2]); float r2=re*a-im*b, i2=re*b+im*a; re=r2; im=i2; }
    Y[k1*DF_+k2]=make_float2(re,im);
  }
}
__global__ void gemm1_k(const uint16_t* xn, const uint16_t* wg, const uint16_t* wu, uint16_t* h){
  int row=blockIdx.x; __shared__ float xs[D_];
  for(int i=threadIdx.x;i<D_;i+=blockDim.x) xs[i]=bf2f(xn[row*D_+i]);
  __syncthreads();
  for(int f=threadIdx.x;f<F_;f+=blockDim.x){
    float g=0,u=0;
    for(int k=0;k<D_;k++){ g+=xs[k]*bf2f(wg[k*F_+f]); u+=xs[k]*bf2f(wu[k*F_+f]); }
    g=rbf(g); u=rbf(u);
    float ge=0.5f*g*(1.0f+tanhf(0.7978845608028654f*(g+0.044715f*g*g*g)));
    ge=rbf(ge);
    h[row*F_+f]=f2bf(ge*u);
  }
}
__global__ void final_k(const uint16_t* x, const uint16_t* h, const uint16_t* wd, const float2* Z, uint16_t* out){
  int row=blockIdx.x; __shared__ float hs[F_]; __shared__ float2 zs[DF_];
  for(int i=threadIdx.x;i<F_;i+=blockDim.x) hs[i]=bf2f(h[row*F_+i]);
  for(int i=threadIdx.x;i<DF_;i+=blockDim.x) zs[i]=Z[row*DF_+i];
  __syncthreads();
  for(int d=threadIdx.x;d<D_;d+=blockDim.x){
    float o=0;
    for(int f=0;f<F_;f++) o+=hs[f]*bf2f(wd[f*D_+d]);
    o=rbf(o);
    float g=rbf(o/(1.0f+expf(-o)));
    float s=zs[0].x + ((d&1)?-zs[256].x:zs[256].x);
    float acc=0;
    for(int k=1;k<256;k++){ int p=(d*k)&(D_-1); float sn,cs; sincospif(2.0f*p/D_,&sn,&cs); acc+=zs[k].x*cs-zs[k].y*sn; }
    s=(s+2.0f*acc)*(1.0f/(S_*D_));
    s=rbf(s);
    float sg=rbf(s*g);
    out[row*D_+d]=f2bf(bf2f(x[row*D_+d])+sg);
  }
}

extern "C" void kernel_launch(void* const* d_in, const int* in_sizes, int n_in,
                              void* d_out, int out_size, void* d_ws, size_t ws_size,
                              hipStream_t stream) {
  const uint16_t* x=(const uint16_t*)d_in[0];
  const uint16_t* wr=(const uint16_t*)d_in[1];
  const uint16_t* wi=(const uint16_t*)d_in[2];
  const uint16_t* wg=(const uint16_t*)d_in[4];
  const uint16_t* wu=(const uint16_t*)d_in[5];
  const uint16_t* wd=(const uint16_t*)d_in[6];
  char* ws=(char*)d_ws;
  uint16_t* xn=(uint16_t*)ws;
  float2* X=(float2*)(ws+(4<<20));
  float2* Y=(float2*)(ws+(12<<20));
  float2* Z=(float2*)(ws+(20<<20));
  uint16_t* h=(uint16_t*)(ws+(28<<20));
  ln_k<<<S_,64,0,stream>>>(x,xn);
  rowdft_k<<<S_,320,0,stream>>>(xn,X);
  coldft_k<<<DF_,1024,0,stream>>>(X,Y,wr,wi,-1.0f);
  coldft_k<<<DF_,1024,0,stream>>>(Y,Z,nullptr,nullptr,1.0f);
  gemm1_k<<<S_,256,0,stream>>>(xn,wg,wu,h);
  final_k<<<S_,256,0,stream>>>(x,h,wd,Z,(uint16_t*)d_out);
}
```

```cpp
#include <hip/hip_runtime.h>
#include <stdint.h>

#define MULTI_LAUNCH 1

#define DI __device__ __forceinline__
#define LAS __attribute__((address_space(3)))
typedef __attribute__((ext_vector_type(2))) float cf;
typedef __attribute__((ext_vector_type(4))) float f32x4;
typedef __attribute__((ext_vector_type(16))) float f32x16;
typedef __attribute__((ext_vector_type(8))) short bf16x8;
typedef __attribute__((ext_vector_type(4))) short s16x4;
typedef __attribute__((ext_vector_type(2))) __bf16 bf16x2;
typedef __attribute__((ext_vector_type(4))) unsigned u32x4;
typedef __attribute__((ext_vector_type(2))) unsigned u32x2;
typedef unsigned long long u64;

constexpr int S_ = 2048, D_ = 512, F_ = 1376, DF_ = 257;
constexpr unsigned WS_EPOCH = 0;
constexpr unsigned WS_FA = 4096, WS_FB = 8192, WS_FC = 12288, WS_FD = 16384;
constexpr unsigned WS_STATS = 32768;
constexpr unsigned WS_W2 = 65536;
constexpr unsigned WS_X = 1u << 20;
constexpr unsigned WS_Z = 5u << 20;
constexpr unsigned WS_W = 9u << 20;
constexpr unsigned WS_S = 11u << 20;
constexpr unsigned WS_H = 13u << 20;
constexpr unsigned WS_END = 20u << 20;
constexpr unsigned L_SLOT = 40960;
constexpr unsigned L_FFT = 143360;
constexpr unsigned L_STAT = L_FFT + 18688;
constexpr unsigned L_CTL = L_STAT + 1024;
constexpr unsigned L_TOTAL = L_CTL + 256;
static_assert(L_TOTAL <= 163840, "LDS");
constexpr int EP = 292, KP = 36;
enum { C_FULL = 0, C_FSYNC = 1, C_FPUB = 2, C_GPUB = 3, C_AREADY = 4, C_CREADY = 5, C_HREADY = 6, C_SXREADY = 7,
       C_WD = 8, C_RED = 9, C_ABORT = 10, C_PRD = 11, C_Q0 = 12  , C_Q1 = 16  , C_MM = 20 };
enum { PH_P0 = 1, PH_G1 = 2, PH_P2 = 4, PH_P3 = 8, PH_G2 = 16, PH_WAIT = 32 };
constexpr unsigned SPIN_CAP = 1u << 16;

struct Args {
  const uint16_t *x, *wr, *wi, *wg, *wu, *wd;
  uint16_t* out;
  char* ws;
};

DI float bflo(unsigned u) { return __uint_as_float(u << 16); }
DI float bfhi(unsigned u) { return __uint_as_float(u & 0xffff0000u); }
DI unsigned pk2(float a, float b) { cf v = {a, b}; bf16x2 c = __builtin_convertvector(v, bf16x2); return __builtin_bit_cast(unsigned, c); }
DI float rbf(float a) { return bflo(pk2(a, a)); }
DI cf cmul(cf a, cf b) { return cf{a.x * b.x - a.y * b.y, a.x * b.y + a.y * b.x}; }
DI cf cmulc(cf a, cf b) { return cf{a.x * b.x + a.y * b.y, a.y * b.x - a.x * b.y}; }
DI cf cis(float rev) { return cf{__builtin_amdgcn_cosf(rev), __builtin_amdgcn_sinf(rev)}; }
template <int INV> DI cf rot(cf a) { return INV ? cf{-a.y, a.x} : cf{a.y, -a.x}; }
template <int INV> DI cf tw(cf a, cf w) { return INV ? cmulc(a, w) : cmul(a, w); }
template <int INV> DI void dft4(cf& y0, cf& y1, cf& y2, cf& y3) {
  cf c0 = y0 + y2, c1 = y1 + y3, d0 = y0 - y2, d1 = rot<INV>(y1 - y3);
  y0 = c0 + c1; y2 = c0 - c1; y1 = d0 + d1; y3 = d0 - d1;
}
template <int INV> DI void dft8(cf (&v)[8]) {
  constexpr float R = 0.70710678118654752f;
  cf a0 = v[0] + v[4], a1 = v[1] + v[5], a2 = v[2] + v[6], a3 = v[3] + v[7];
  cf b0 = v[0] - v[4], t1 = v[1] - v[5], b2 = rot<INV>(v[2] - v[6]), t3 = v[3] - v[7];
  cf b1 = (t1 + rot<INV>(t1)) * R;
  cf b3 = (rot<INV>(t3) - t3) * R;
  dft4<INV>(a0, a1, a2, a3);
  dft4<INV>(b0, b1, b2, b3);
  v[0] = a0; v[2] = a1; v[4] = a2; v[6] = a3;
  v[1] = b0; v[3] = b1; v[5] = b2; v[7] = b3;
}
template <int CTRL> DI float dppf(float v) { return __int_as_float(__builtin_amdgcn_update_dpp(0, __float_as_int(v), CTRL, 0xf, 0xf, true)); }
template <int CTRL> DI unsigned dppu(unsigned v) { return (unsigned)__builtin_amdgcn_update_dpp(0, (int)v, CTRL, 0xf, 0xf, true); }
template <int CTRL> DI void swap2(cf& a, cf& b, bool bit) {
  float pax = dppf<CTRL>(a.x), pay = dppf<CTRL>(a.y), pbx = dppf<CTRL>(b.x), pby = dppf<CTRL>(b.y);
  cf an = {bit ? pbx : a.x, bit ? pby : a.y}, bn = {bit ? b.x : pax, bit ? b.y : pay};
  a = an; b = bn;
}
template <int CTRL> DI void swap2u(unsigned& a, unsigned& b, bool bit) {
  unsigned pa = dppu<CTRL>(a), pb = dppu<CTRL>(b);
  unsigned an = bit ? pb : a, bn = bit ? b : pa;
  a = an; b = bn;
}
DI void quadT(cf& r0, cf& r1, cf& r2, cf& r3, bool b0, bool b1) {
  swap2<0xB1>(r0, r1, b0); swap2<0xB1>(r2, r3, b0);
  swap2<0x4E>(r0, r2, b1); swap2<0x4E>(r1, r3, b1);
}
DI void quadTu(unsigned& r0, unsigned& r1, unsigned& r2, unsigned& r3, bool b0, bool b1) {
  swap2u<0xB1>(r0, r1, b0); swap2u<0xB1>(r2, r3, b0);
  swap2u<0x4E>(r0, r2, b1); swap2u<0x4E>(r1, r3, b1);
}
DI float red32(float v) {
  v += dppf<0xB1>(v); v += dppf<0x4E>(v); v += dppf<0x141>(v); v += dppf<0x140>(v);
  auto r = __builtin_amdgcn_permlane16_swap(__float_as_uint(v), __float_as_uint(v), false, false);
  return __uint_as_float(r[0]) + __uint_as_float(r[1]);
}

DI LAS char* ldsp(unsigned off) { return (LAS char*)(uintptr_t)off; }
DI cf lds_ld(unsigned off) { return *(LAS cf*)ldsp(off); }
DI void lds_st(unsigned off, cf v) { *(LAS cf*)ldsp(off) = v; }
DI void wait_lgkm() { asm volatile("s_waitcnt lgkmcnt(0)" ::: "memory"); }
template <int N> DI void wait_vm() { asm volatile("s_waitcnt vmcnt(%0)" ::"i"(N) : "memory"); }
DI void ctl_add(unsigned ctl, int idx, int lane) {
  wait_lgkm();
  if (lane == 0) __hip_atomic_fetch_add((LAS unsigned*)ldsp(ctl + 4 * idx), 1u, __ATOMIC_RELAXED, __HIP_MEMORY_SCOPE_WORKGROUP);
  asm volatile("" ::: "memory");
}
DI unsigned ctl_add_rtn(unsigned ctl, int idx, int lane) {
  wait_lgkm();
  unsigned r = 0;
  if (lane == 0) r = __hip_atomic_fetch_add((LAS unsigned*)ldsp(ctl + 4 * idx), 1u, __ATOMIC_RELAXED, __HIP_MEMORY_SCOPE_WORKGROUP);
  asm volatile("" ::: "memory");
  return __builtin_amdgcn_readfirstlane(r);
}
DI void ctl_set(unsigned ctl, int idx, unsigned v, int lane) {
  wait_lgkm();
  if (lane == 0) *(volatile LAS unsigned*)ldsp(ctl + 4 * idx) = v;
  asm volatile("" ::: "memory");
}
template <int SLEEP> DI void ctl_wait(unsigned ctl, int idx, unsigned target) {
  for (unsigned spins = 0;; ++spins) {
    asm volatile("" ::: "memory");
    u32x2 v;
    v.x = *(volatile LAS unsigned*)ldsp(ctl + 4 * idx);
    const unsigned c = __builtin_amdgcn_readfirstlane(v.x);
    if (c >= target) break;
    if ((spins & 63u) == 63u) {
      v.y = *(volatile LAS unsigned*)ldsp(ctl + 4 * C_ABORT);
      if (__builtin_amdgcn_readfirstlane(v.y) != 0u) break;
      if (spins > (SPIN_CAP << 4)) { *(volatile LAS unsigned*)ldsp(ctl + 4 * C_ABORT) = 1u; break; }
    }
    if (SLEEP) __builtin_amdgcn_s_sleep(SLEEP);
  }
  asm volatile("" ::: "memory");
}
DI void glds(unsigned voff, const void* sbase, unsigned ldsdst) {
  unsigned keep;
  asm volatile("s_mov_b32 %0, m0\n\ts_mov_b32 m0, %3\n\ts_nop 0\n\tglobal_load_lds_dwordx4 %1, %2\n\ts_mov_b32 m0, %0"
               : "=&s"(keep) : "v"(voff), "s"(sbase), "s"(ldsdst) : "memory");
}
typedef __amdgpu_buffer_rsrc_t rsrc_t;
DI u32x4 ld16(rsrc_t r, unsigned off) { return __builtin_amdgcn_raw_buffer_load_b128(r, off, 0, 16); }
DI u32x2 ld8(rsrc_t r, unsigned off) { return __builtin_amdgcn_raw_buffer_load_b64(r, off, 0, 16); }
DI unsigned ld4(rsrc_t r, unsigned off) { return __builtin_amdgcn_raw_buffer_load_b32(r, off, 0, 16); }
DI void st16(rsrc_t r, unsigned off, u32x4 v) { __builtin_amdgcn_raw_buffer_store_b128(v, r, off, 0, 16); }
DI void st8(rsrc_t r, unsigned off, u32x2 v) { __builtin_amdgcn_raw_buffer_store_b64(v, r, off, 0, 16); }
DI void st4(rsrc_t r, unsigned off, unsigned v) { __builtin_amdgcn_raw_buffer_store_b32(v, r, off, 0, 16); }
DI u32x4 cc2(cf a, cf b) { return u32x4{__float_as_uint(a.x), __float_as_uint(a.y), __float_as_uint(b.x), __float_as_uint(b.y)}; }

DI void publish(rsrc_t ws, unsigned ctl, int cidx, unsigned target, unsigned flag_off, u32x2 tag, int lane) {
  wait_vm<0>();
  const unsigned r = ctl_add_rtn(ctl, cidx, lane);
  if (r + 1 == target && lane == 0) st8(ws, flag_off, tag);
}
DI void poll_flags(rsrc_t ws, unsigned ctl, unsigned flag_off, int nflags, u32x2 tag, int lane) {
  const unsigned o = flag_off + ((4 * lane < nflags) ? 32u * lane : 0u);
  for (unsigned spins = 0;; ++spins) {
    asm volatile("" ::: "memory");
    const u32x4 a = ld16(ws, o), b = ld16(ws, o + 16);
    const bool ok = a.x == tag.x && a.y == tag.y && a.z == tag.x && a.w == tag.y && b.x == tag.x && b.y == tag.y && b.z == tag.x && b.w == tag.y;
    if (__all(ok)) break;
    if (spins > SPIN_CAP) { ctl_set(ctl, C_ABORT, 1u, lane); break; }
    if ((spins & 15u) == 15u && __builtin_amdgcn_readfirstlane(*(volatile LAS unsigned*)ldsp(ctl + 4 * C_ABORT)) != 0u) break;
    __builtin_amdgcn_s_sleep(2);
  }
  asm volatile("" ::: "memory");
}

DI void fft_role(const Args& A, int mode, int t, rsrc_t ws, u32x2 tag, u32x2 epoch) {
  const int lane = t & 63, w = __builtin_amdgcn_readfirstlane(t >> 6), bid = blockIdx.x;
  const unsigned ctl = L_CTL;
  const bool WAIT = mode & PH_WAIT;
  const int rowi = 2 * w + (lane >> 5);
  const int l5 = lane & 31, k1l = (lane >> 2) & 7, llo = lane & 3;
  const bool b0 = lane & 1, b1 = lane & 2;
  unsigned fs = 0;
  auto fsync = [&]() { fs += 4; ctl_add(ctl, C_FSYNC, lane); ctl_wait<0>(ctl, C_FSYNC, fs); };
  const unsigned aW1 = L_FFT + 8u * (rowi * EP + l5);
  const unsigned aR1 = L_FFT + 8u * (rowi * EP + k1l * KP + llo);
  const unsigned aK = L_FFT + 8u * (rowi * EP + k1l + 8 * llo);
  cf twA[7], tw2[7];
#pragma unroll
  for (int i = 1; i < 8; ++i) { twA[i - 1] = cis(-(float)(l5 * i) * (1.0f / 256.0f)); tw2[i - 1] = cis(-(float)(llo * i) * (1.0f / 32.0f)); }
  int jj[2], sd[2];
#pragma unroll
  for (int p = 0; p < 2; ++p) {
    const int k = t;
    if ((k & 127) == 0) { jj[p] = k >> 7; sd[p] = p; }
    else if (k < 128) { jj[p] = 2 * k + p; sd[p] = 0; }
    else { jj[p] = 2 * (256 - k) + p; sd[p] = 1; }
  }
  cf v[8];
  if (mode & PH_P0) {
    const int row = bid + 256 * rowi;
    unsigned raw[8];
    const unsigned* xr = (const unsigned*)(A.x + (size_t)row * D_);
#pragma unroll
    for (int i = 0; i < 8; ++i) raw[i] = xr[l5 + 32 * i];
    unsigned wq[8];
#pragma unroll
    for (int c = 0; c < 8; ++c) {
      const int o = (8 * bid + c) * DF_ + t;
      wq[c] = (unsigned)A.wr[o] | ((unsigned)A.wi[o] << 16);
    }
    unsigned w256 = 0;
    if (t < 8) { const int o = (8 * bid + t) * DF_ + 256; w256 = (unsigned)A.wr[o] | ((unsigned)A.wi[o] << 16); }
    float s = 0.f;
#pragma unroll
    for (int i = 0; i < 8; ++i) { v[i] = cf{bflo(raw[i]), bfhi(raw[i])}; s += v[i].x + v[i].y; }
    const float mean = red32(s) * (1.0f / D_);
    float q = 0.f;
#pragma unroll
    for (int i = 0; i < 8; ++i) { const float dx = v[i].x - mean, dy = v[i].y - mean; q += dx * dx + dy * dy; }
    const float var = rbf(red32(q) * (1.0f / D_));
    const float mb = rbf(mean);
    const float den = rbf(sqrtf(rbf(var + 1e-5f)));
    const float rinv = 1.0f / den;
#pragma unroll
    for (int i = 0; i < 8; ++i) {
      const unsigned d2 = pk2(v[i].x - mb, v[i].y - mb);
      const unsigned n2 = pk2(bflo(d2) * rinv, bfhi(d2) * rinv);
      v[i] = cf{bflo(n2), bfhi(n2)};
    }
    if (l5 == 0) st8(ws, WS_STATS + 8u * row, u32x2{__float_as_uint(mb), __float_as_uint(rinv)});
#pragma unroll
    for (int p = 0; p < 2; ++p)
      st16(ws, WS_W + (unsigned)jj[p] * 8192u + (unsigned)bid * 32u + (unsigned)sd[p] * 16u, u32x4{wq[p], wq[2 + p], wq[4 + p], wq[6 + p]});
    if (t < 8) st4(ws, WS_W2 + 4u * (8 * bid + (t & 1) * 4 + (t >> 1)), w256);
    dft8<0>(v);
#pragma unroll
    for (int i = 1; i < 8; ++i) v[i] = cmul(v[i], twA[i - 1]);
#pragma unroll
    for (int i = 0; i < 8; ++i) lds_st(aW1 + 8u * KP * i, v[i]);
    wait_lgkm();
#pragma unroll
    for (int i = 0; i < 8; ++i) v[i] = lds_ld(aR1 + 32u * i);
    dft8<0>(v);
#pragma unroll
    for (int i = 1; i < 8; ++i) v[i] = cmul(v[i], tw2[i - 1]);
    quadT(v[0], v[1], v[2], v[3], b0, b1);
    quadT(v[4], v[5], v[6], v[7], b0, b1);
    dft4<0>(v[0], v[1], v[2], v[3]);
    dft4<0>(v[4], v[5], v[6], v[7]);
    wait_lgkm();
#pragma unroll
    for (int i = 0; i < 8; ++i) lds_st(aK + 8u * (32 * (i >> 2) + 64 * (i & 3)), v[i]);
    fsync();
#pragma unroll
    for (int i = 0; i < 8; ++i) v[i] = lds_ld(L_FFT + 8u * (i * EP + t));
    dft8<0>(v);
#pragma unroll
    for (int c = 1; c < 8; ++c) v[c] = cmul(v[c], cis(-(float)(bid * c) * (1.0f / 2048.0f)));
#pragma unroll
    for (int p = 0; p < 2; ++p) {
      const unsigned o = WS_X + (unsigned)jj[p] * 16384u + (unsigned)bid * 64u + (unsigned)sd[p] * 32u;
      st16(ws, o, cc2(v[p], v[2 + p]));
      st16(ws, o + 16, cc2(v[4 + p], v[6 + p]));
    }
    if (WAIT) publish(ws, ctl, C_FPUB, 4, WS_FA + 8u * bid, tag, lane);
  }
  if (WAIT) {
    if (w == 0) {
      poll_flags(ws, ctl, WS_FA, 256, tag, lane);
      ctl_set(ctl, C_AREADY, 1u, lane);
      if (bid == 0 && lane == 0) {
        const u64 e1 = (((u64)epoch.y << 32) | epoch.x) + 1ull;
        st8(ws, WS_EPOCH, u32x2{(unsigned)e1, (unsigned)(e1 >> 32)});
      }
    } else ctl_wait<1>(ctl, C_AREADY, 1u);
  }
  if (mode & PH_G1) {
    const int mt = (bid & 1) * 8 + ((bid >> 3) & 7);
    if (w == 3) {
      const u32x4 sv = ld16(ws, WS_STATS + 8u * (mt * 128) + 16u * lane);
      *(LAS u32x4*)ldsp(L_STAT + 16u * lane) = sv;
      ctl_set(ctl, C_MM, 1u, lane);
    }
  }
  if (mode & PH_P2) {
    const int j = bid, e = t & 7, p5 = t >> 3;
    const unsigned xo = WS_X + (unsigned)j * 16384u + 8u * t;
#pragma unroll
    for (int i = 0; i < 8; ++i) { const u32x2 r = ld8(ws, xo + 2048u * i); v[i] = cf{__uint_as_float(r.x), __uint_as_float(r.y)}; }
    const int sig = rowi >> 2, idx = rowi & 3;
    const bool self = j < 2;
    const int par = self ? sig : (j & 1);
    const int c = 2 * idx + par;
    const int idxp = par ? 3 - idx : ((4 - idx) & 3);
    const int ep = (self ? sig : 1 - sig) * 4 + idxp;
    const int kcol = self ? 128 * j : (sig ? 256 - (j >> 1) : (j >> 1));
    const int dbase = k1l + 8 * llo;
    const int dneg = 256 - (c != 0 ? 1 : 0);
    unsigned w1[8], w2[8];
#pragma unroll
    for (int i = 0; i < 8; ++i) {
      const int d = dbase + 32 * (i >> 2) + 64 * (i & 3), dp = (dneg - d) & 255;
      w1[i] = ld4(ws, WS_W + (unsigned)j * 8192u + 32u * d + 4u * rowi);
      w2[i] = ld4(ws, WS_W + (unsigned)j * 8192u + 32u * dp + 4u * ep);
    }
    cf twB[7];
#pragma unroll
    for (int i = 1; i < 8; ++i) twB[i - 1] = cis(-(float)(p5 * i) * (1.0f / 256.0f));
    const unsigned aB = L_FFT + 8u * (e * EP + p5);
    dft8<0>(v);
#pragma unroll
    for (int i = 1; i < 8; ++i) v[i] = cmul(v[i], twB[i - 1]);
    if (mode & PH_P0) fsync();
#pragma unroll
    for (int i = 0; i < 8; ++i) lds_st(aB + 8u * KP * i, v[i]);
    fsync();
#pragma unroll
    for (int i = 0; i < 8; ++i) v[i] = lds_ld(aR1 + 32u * i);
    dft8<0>(v);
#pragma unroll
    for (int i = 1; i < 8; ++i) v[i] = cmul(v[i], tw2[i - 1]);
    quadT(v[0], v[1], v[2], v[3], b0, b1);
    quadT(v[4], v[5], v[6], v[7], b0, b1);
    dft4<0>(v[0], v[1], v[2], v[3]);
    dft4<0>(v[4], v[5], v[6], v[7]);
    wait_lgkm();
#pragma unroll
    for (int i = 0; i < 8; ++i) lds_st(aK + 8u * (32 * (i >> 2) + 64 * (i & 3)), v[i]);
    fsync();
    cf qv[8];
#pragma unroll
    for (int i = 0; i < 8; ++i) {
      const int d = dbase + 32 * (i >> 2) + 64 * (i & 3), dp = (dneg - d) & 255;
      qv[i] = lds_ld(L_FFT + 8u * (ep * EP + dp));
    }
    ctl_add(ctl, C_PRD, lane);
    const cf tt = cis(-(float)kcol * (1.0f / 512.0f));
    constexpr float SC = 0.25f / (2048.0f * 256.0f);
    cf W1[8], W2[8];
#pragma unroll
    for (int i = 0; i < 8; ++i) { W1[i] = cf{bflo(w1[i]), bfhi(w1[i])}; W2[i] = cf{bflo(w2[i]), -bfhi(w2[i])}; }
    if (j == 0) {
#pragma unroll
      for (int i = 0; i < 8; ++i) {
        const int d = dbase + 32 * (i >> 2) + 64 * (i & 3), dp = (dneg - d) & 255;
        const unsigned u1 = ld4(ws, WS_W2 + 4u * (8 * d + rowi)), u2 = ld4(ws, WS_W2 + 4u * (8 * dp + ep));
        W1[i] = (W1[i] + W2[i]) * 0.5f;
        W2[i] = cf{bflo(u1) + bflo(u2), bfhi(u1) - bfhi(u2)} * 0.5f;
      }
    }
#pragma unroll
    for (int i = 0; i < 8; ++i) {
      const cf P = v[i], Qc = cf{qv[i].x, -qv[i].y};
      const cf E = P + Qc, T = cmul(rot<0>(P - Qc), tt);
      const cf Y1 = cmul(E + T, W1[i]), Y2 = cmul(E - T, W2[i]);
      v[i] = ((Y1 + Y2) + rot<1>(cmulc(Y1 - Y2, tt))) * SC;
    }
    dft4<1>(v[0], v[1], v[2], v[3]);
    dft4<1>(v[4], v[5], v[6], v[7]);
    quadT(v[0], v[1], v[2], v[3], b0, b1);
    quadT(v[4], v[5], v[6], v[7], b0, b1);
#pragma unroll
    for (int i = 1; i < 8; ++i) v[i] = cmulc(v[i], tw2[i - 1]);
    dft8<1>(v);
    ctl_wait<0>(ctl, C_PRD, 4u);
#pragma unroll
    for (int i = 0; i < 8; ++i) lds_st(aR1 + 32u * i, v[i]);
    fsync();
#pragma unroll
    for (int i = 0; i < 8; ++i) v[i] = lds_ld(aB + 8u * KP * i);
#pragma unroll
    for (int i = 1; i < 8; ++i) v[i] = cmulc(v[i], twB[i - 1]);
    dft8<1>(v);
#pragma unroll
    for (int i = 0; i < 8; i += 2) {
      swap2<0xB1>(v[i], v[i + 1], b0);
      const int b = p5 + 32 * (i + (b0 ? 1 : 0));
      st16(ws, WS_Z + (unsigned)b * 16384u + (unsigned)j * 64u + 8u * (e & 6), cc2(v[i], v[i + 1]));
    }
    if (WAIT) publish(ws, ctl, C_FPUB, 8, WS_FC + 8u * bid, tag, lane);
  }
  if (WAIT) {
    if (w == 0) { poll_flags(ws, ctl, WS_FC, 256, tag, lane); ctl_set(ctl, C_CREADY, 1u, lane); }
    else ctl_wait<1>(ctl, C_CREADY, 1u);
  }
  if (mode & PH_P3) {
#pragma unroll
    for (int p = 0; p < 2; ++p) {
      const unsigned o = WS_Z + (unsigned)bid * 16384u + (unsigned)jj[p] * 64u + (unsigned)sd[p] * 32u;
      const u32x4 r0 = ld16(ws, o), r1 = ld16(ws, o + 16);
      v[p] = cf{__uint_as_float(r0.x), __uint_as_float(r0.y)}; v[2 + p] = cf{__uint_as_float(r0.z), __uint_as_float(r0.w)};
      v[4 + p] = cf{__uint_as_float(r1.x), __uint_as_float(r1.y)}; v[6 + p] = cf{__uint_as_float(r1.z), __uint_as_float(r1.w)};
    }
#pragma unroll
    for (int c = 1; c < 8; ++c) v[c] = cmulc(v[c], cis(-(float)(bid * c) * (1.0f / 2048.0f)));
    dft8<1>(v);
    if (mode & PH_P2) fsync();
#pragma unroll
    for (int i = 0; i < 8; ++i) lds_st(L_FFT + 8u * (i * EP + t), v[i]);
    fsync();
#pragma unroll
    for (int i = 0; i < 8; ++i) v[i] = lds_ld(aK + 8u * (32 * (i >> 2) + 64 * (i & 3)));
    dft4<1>(v[0], v[1], v[2], v[3]);
    dft4<1>(v[4], v[5], v[6], v[7]);
    quadT(v[0], v[1], v[2], v[3], b0, b1);
    quadT(v[4], v[5], v[6], v[7], b0, b1);
#pragma unroll
    for (int i = 1; i < 8; ++i) v[i] = cmulc(v[i], tw2[i - 1]);
    dft8<1>(v);
    wait_lgkm();
#pragma unroll
    for (int i = 0; i < 8; ++i) lds_st(aR1 + 32u * i, v[i]);
    wait_lgkm();
#pragma unroll
    for (int i = 0; i < 8; ++i) v[i] = lds_ld(aW1 + 8u * KP * i);
#pragma unroll
    for (int i = 1; i < 8; ++i) v[i] = cmulc(v[i], twA[i - 1]);
    dft8<1>(v);
    unsigned o[8];
#pragma unroll
    for (int i = 0; i < 8; ++i) o[i] = pk2(v[i].x, v[i].y);
    quadTu(o[0], o[1], o[2], o[3], b0, b1);
    quadTu(o[4], o[5], o[6], o[7], b0, b1);
    const int row = bid + 256 * rowi;
    const unsigned so = WS_S + (unsigned)row * 1024u + 4u * ((l5 & ~3) + 32 * llo);
    st16(ws, so, u32x4{o[0], o[1], o[2], o[3]});
    st16(ws, so + 512, u32x4{o[4], o[5], o[6], o[7]});
    if (WAIT) publish(ws, ctl, C_FPUB, 12, WS_FD + 8u * bid, tag, lane);
  }
  if (mode & PH_G2) {
    const int mt2 = (bid & 7) * 4 + ((bid >> 3) & 3), nt2 = bid >> 5;
    const int tok = t >> 2, seg = t & 3;
    const u32x4* xp = (const u32x4*)(A.x + (size_t)(mt2 * 64 + tok) * D_ + nt2 * 64 + seg * 16);
    const u32x4 x0 = xp[0], x1 = xp[1];
    if (WAIT) {
      if (w == 0) { poll_flags(ws, ctl, WS_FD + 8u * ((mt2 * 64) & 255), 64, tag, lane); ctl_set(ctl, C_CREADY, 2u, lane); }
      else ctl_wait<1>(ctl, C_CREADY, 2u);
    }
    const unsigned so = WS_S + (unsigned)(mt2 * 64 + tok) * 1024u + (unsigned)nt2 * 128u + 32u * seg;
    const u32x4 s0 = ld16(ws, so), s1 = ld16(ws, so + 16);
    if (mode & PH_P3) fsync();
    const unsigned lo = L_FFT + 32u * t;
    *(LAS u32x4*)ldsp(lo) = x0; *(LAS u32x4*)ldsp(lo + 16) = x1;
    *(LAS u32x4*)ldsp(lo + 8192) = s0; *(LAS u32x4*)ldsp(lo + 8192 + 16) = s1;
    ctl_add(ctl, C_SXREADY, lane);
  }
}

DI bf16x8 tr2(unsigned a0, unsigned a1) {
  const s16x4 lo = __builtin_amdgcn_ds_read_tr16_b64_v4i16((LAS s16x4*)ldsp(a0));
  const s16x4 hi = __builtin_amdgcn_ds_read_tr16_b64_v4i16((LAS s16x4*)ldsp(a1));
  return __builtin_shufflevector(lo, hi, 0, 1, 2, 3, 4, 5, 6, 7);
}
DI float dot1(unsigned u, float acc) {
  const unsigned o = 0x3F803F80u;
  return __builtin_amdgcn_fdot2_f32_bf16(__builtin_bit_cast(bf16x2, u), __builtin_bit_cast(bf16x2, o), acc, false);
}
DI float dot_ones(bf16x8 f, float acc) {
  const u32x4 u = __builtin_bit_cast(u32x4, f);
  const unsigned a = u.x, b = u.y, c = u.z, d = u.w;
  return dot1(d, dot1(c, dot1(b, dot1(a, acc))));
}
#define MFMA(a, b, c) __builtin_amdgcn_mfma_f32_32x32x16_bf16((a), (b), (c), 0, 0, 0)

DI void wd_piece(const Args& A, int pi, int nt2, unsigned voff) {
  const int sg = pi >> 1, kq = sg / 17, i = sg - 17 * kq, u = 4 * i + kq;
  glds(voff, (const char*)A.wd + (size_t)(16 * u + 8 * (pi & 1)) * 1024 + nt2 * 128, (unsigned)pi * 1024u);
}

DI void gemm_role(const Args& A, int mode, int t, rsrc_t ws, u32x2 tag) {
  const int lane = t & 63, w = __builtin_amdgcn_readfirstlane(t >> 6), bid = blockIdx.x;
  const unsigned ctl = L_CTL;
  const bool WAIT = mode & PH_WAIT;
  const int r = lane & 31, h = lane >> 5;
  const int g4 = lane >> 4, q4 = (lane >> 2) & 3, pp = lane & 3;
  const int mt2 = (bid & 7) * 4 + ((bid >> 3) & 3), nt2 = bid >> 5;
  const unsigned wdv = (unsigned)(lane >> 3) * 1024u + 16u * ((lane & 7) ^ (((lane >> 4) & 1) << 2));
  if (mode & PH_G1) {
    const int tg = w & 3, fg = w >> 2;
    const int mt = (bid & 1) * 8 + ((bid >> 3) & 7), nt = ((bid >> 1) & 3) * 4 + (bid >> 6);
    const int np = ((nt & 3) == 3 || nt == 14) ? 2 : 3;
    const int start = 3 * nt - (nt >> 2) - (nt == 15 ? 1 : 0);
    unsigned xv[2], wv[3]; bool wok[3];
#pragma unroll
    for (int i = 0; i < 2; ++i) { const int row = (2 * w + i) * 8 + (lane >> 3); xv[i] = (unsigned)row * 1024u + 16u * ((lane & 7) ^ ((row >> 1) & 7)); }
#pragma unroll
    for (int i = 0; i < 3; ++i) {
      const int cell = 4 * ((w & 3) * 3 + i) + g4, a = cell / 3, pair = cell - 3 * a;
      wv[i] = (unsigned)(4 * a + q4) * 2752u + (unsigned)pair * 64u + 16u * (pp ^ (2 * fg));
      wok[i] = pair < np;
    }
    const char* xb = (const char*)A.x + (size_t)mt * 128 * 1024;
    const char* wb = (const char*)(fg ? A.wu : A.wg) + start * 64;
    auto issue = [&](int s, unsigned slot) {
#pragma unroll
      for (int i = 0; i < 2; ++i) glds(xv[i], xb + s * 128, slot + (unsigned)(2 * w + i) * 1024u);
#pragma unroll
      for (int i = 0; i < 3; ++i)
        if (wok[i]) glds(wv[i], wb + (size_t)s * 64 * 2752, slot + 16384u + (unsigned)fg * 12288u + (unsigned)((w & 3) * 3 + i) * 1024u);
    };
    issue(0, 0); issue(1, L_SLOT); issue(2, 2 * L_SLOT);
    __builtin_amdgcn_s_barrier();
    const int xrow = tg * 32 + r;
    const unsigned xa = (unsigned)xrow * 128u + 16u * (h ^ ((xrow >> 1) & 7));
    const int mat = g4 & 1;
    const unsigned wa = 16384u + (unsigned)mat * 12288u + (unsigned)(2 * (g4 >> 1)) * 768u + (unsigned)q4 * 64u + (unsigned)(fg ^ mat) * 32u + 8u * pp;
    f32x16 acc[3];
    float cs[3] = {0.f, 0.f, 0.f};
#pragma unroll
    for (int p = 0; p < 3; ++p)
#pragma unroll
      for (int i = 0; i < 16; ++i) acc[p][i] = 0.f;
    unsigned slot = 0;
    wait_vm<10>();
    ctl_add(ctl, C_FULL, lane);
    for (int s = 0; s < 8; ++s) {
      ctl_wait<0>(ctl, C_FULL, 8u * (s + 1));
      if (s >= 1) {
        const unsigned pslot = slot == 0 ? 2 * L_SLOT : slot - L_SLOT;
        if (s + 2 < 8) issue(s + 2, pslot);
        else if (mode & PH_G2) {
          const int p0 = (s == 6 ? 80 : 0) + 5 * w;
#pragma unroll
          for (int i = 0; i < 5; ++i) wd_piece(A, p0 + i, nt2, wdv);
        }
      }
      const unsigned xs = slot + xa, wsl = slot + wa;
#pragma unroll
      for (int ks = 0; ks < 4; ++ks) {
        const bf16x8 xf = *(LAS bf16x8*)ldsp(xs ^ (32u * ks));
        bf16x8 wf[3];
#pragma unroll
        for (int p = 0; p < 3; ++p) wf[p] = tr2(wsl + (4 * ks) * 768u + p * 256u, wsl + (4 * ks + 1) * 768u + p * 256u);
        if (ks == 3) {
          if (s < 7) { if ((mode & PH_G2) || s < 6) wait_vm<5>(); else wait_vm<0>(); }
          ctl_add(ctl, C_FULL, lane);
        }
#pragma unroll
        for (int p = 0; p < 3; ++p) { acc[p] = MFMA(wf[p], xf, acc[p]); cs[p] = dot_ones(wf[p], cs[p]); }
      }
      slot = slot == 2 * L_SLOT ? 0 : slot + L_SLOT;
    }
    ctl_wait<1>(ctl, C_MM, 1u);
    const cf st = lds_ld(L_STAT + 8u * xrow);
    const unsigned nm = pk2(-st.x, -st.x);
    const u32x4 bq = {nm, nm & 0xffffu, 0u, 0u};
    const bf16x8 bfrag = __builtin_bit_cast(bf16x8, bq);
#pragma unroll
    for (int p = 0; p < 3; ++p) {
      if (p < np) {
        const float c0 = rbf(cs[p]), r1 = cs[p] - c0, c1 = rbf(r1), c2 = r1 - c1;
        const u32x4 aq = {pk2(c0, c1), pk2(c2, 0.f), 0u, 0u};
        const f32x16 a = MFMA(__builtin_bit_cast(bf16x8, aq), bfrag, acc[p]);
        unsigned hv[4];
#pragma unroll
        for (int i = 0; i < 4; ++i) {
          const unsigned g2 = pk2(a[2 * i] * st.y, a[2 * i + 1] * st.y), u2 = pk2(a[8 + 2 * i] * st.y, a[9 + 2 * i] * st.y);
          float gg[2] = {bflo(g2), bfhi(g2)}, ge[2];
#pragma unroll
          for (int z = 0; z < 2; ++z) {
            constexpr float K0 = -2.0f * 0.7978845608028654f * 1.4426950408889634f, K1 = K0 * 0.044715f;
            const float g = gg[z], e = __builtin_amdgcn_exp2f(g * (K0 + K1 * g * g));
            ge[z] = g * __builtin_amdgcn_rcpf(1.0f + e);
          }
          const unsigned e2 = pk2(ge[0], ge[1]);
          hv[i] = pk2(bflo(e2) * bflo(u2), bfhi(e2) * bfhi(u2));
        }
        const int unit = 2 * (start + p) + fg;
        st16(ws, WS_H + (unsigned)((mt * 4 + tg) * 86 + unit) * 1024u + 16u * lane, u32x4{hv[0], hv[1], hv[2], hv[3]});
      }
    }
    if (WAIT) publish(ws, ctl, C_GPUB, 8, WS_FB + 8u * (mt * 16 + nt), tag, lane);
    if (mode & PH_G2) {
      ctl_wait<0>(ctl, C_FULL, 72u);
#pragma unroll
      for (int i = 0; i < 5; ++i) wd_piece(A, 40 + 5 * w + i, nt2, wdv);
#pragma unroll
      for (int i = 0; i < 2; ++i) wd_piece(A, 120 + 2 * w + i, nt2, wdv);
    }
  } else {
    __builtin_amdgcn_s_barrier();
    if (mode & PH_G2) {
      for (int i = 0; i < 17; ++i) wd_piece(A, 17 * w + i, nt2, wdv);
    }
  }
  if (mode & PH_G2) {
    const int th = w & 1, kq = w >> 1;
    const int n = kq < 2 ? 22 : 21;
    wait_vm<0>();
    ctl_add(ctl, C_WD, lane);
    if (WAIT) {
      if (w == 0) { poll_flags(ws, ctl, WS_FB + 128u * (mt2 >> 1), 16, tag, lane); ctl_set(ctl, C_HREADY, 1u, lane); }
      else ctl_wait<1>(ctl, C_HREADY, 1u);
    }
    const unsigned ho = WS_H + (unsigned)((2 * mt2 + th) * 86 + kq) * 1024u + 16u * lane;
    u32x4 hf[22];
#pragma unroll
    for (int i = 0; i < 22; ++i) hf[i] = ld16(ws, ho + 4096u * (i == 21 ? n - 1 : i));
    ctl_wait<0>(ctl, C_WD, 8u);
    const int dsub = g4 & 1, hh = g4 >> 1;
    unsigned wa2[2];
#pragma unroll
    for (int dh = 0; dh < 2; ++dh)
      wa2[dh] = (unsigned)kq * 17u * 2048u + (unsigned)(4 * hh + q4) * 128u + (unsigned)(dh ^ (q4 >> 1)) * 64u + (unsigned)dsub * 32u + 8u * pp;
    f32x16 acc[2];
#pragma unroll
    for (int p = 0; p < 2; ++p)
#pragma unroll
      for (int i = 0; i < 16; ++i) acc[p][i] = 0.f;
#pragma unroll
    for (int i = 0; i < 22; ++i) {
      if (i == 7) {
        ctl_wait<0>(ctl, C_Q0 + kq, 2u);
#pragma unroll
        for (int z = 0; z < 5; ++z) {
          const int u = 4 * (17 + z) + kq;
          if (u < 86) glds(wdv, (const char*)A.wd + (size_t)(16 * u + 8 * th) * 1024 + nt2 * 128, (unsigned)(kq * 17 + z) * 2048u + (unsigned)th * 1024u);
        }
      }
      if (i == 17) { wait_vm<0>(); ctl_add(ctl, C_Q1 + kq, lane); ctl_wait<0>(ctl, C_Q1 + kq, 2u); }
      if (i < 21 || n == 22) {
        const unsigned so = (unsigned)(i < 17 ? i : i - 17) * 2048u;
        const bf16x8 wf0 = tr2(wa2[0] + so, wa2[0] + so + 1024u), wf1 = tr2(wa2[1] + so, wa2[1] + so + 1024u);
        if (i == 4) ctl_add(ctl, C_Q0 + kq, lane);
        const bf16x8 hb = __builtin_bit_cast(bf16x8, hf[i]);
        acc[0] = MFMA(wf0, hb, acc[0]);
        acc[1] = MFMA(wf1, hb, acc[1]);
      }
    }
    ctl_add(ctl, C_RED, lane);
    ctl_wait<0>(ctl, C_RED, 8u);
    const unsigned pw = (unsigned)kq * 17408u + (unsigned)(th * 32 + r) * 272u + 16u * h;
#pragma unroll
    for (int dh = 0; dh < 2; ++dh)
#pragma unroll
      for (int g = 0; g < 4; ++g)
        *(LAS f32x4*)ldsp(pw + dh * 128u + g * 32u) = f32x4{acc[dh][4 * g], acc[dh][4 * g + 1], acc[dh][4 * g + 2], acc[dh][4 * g + 3]};
    ctl_add(ctl, C_RED, lane);
    ctl_wait<0>(ctl, C_RED, 16u);
    const int tok = t >> 3, d8 = t & 7;
    const unsigned pr = (unsigned)tok * 272u + 32u * d8;
    f32x4 o0 = *(LAS f32x4*)ldsp(pr), o1 = *(LAS f32x4*)ldsp(pr + 16);
#pragma unroll
    for (int z = 1; z < 4; ++z) { o0 += *(LAS f32x4*)ldsp(pr + z * 17408u); o1 += *(LAS f32x4*)ldsp(pr + z * 17408u + 16); }
    ctl_wait<1>(ctl, C_SXREADY, 4u);
    const u32x4 xq = *(LAS u32x4*)ldsp(L_FFT + 16u * t), sq = *(LAS u32x4*)ldsp(L_FFT + 8192u + 16u * t);
    const float ov[8] = {o0.x, o0.y, o0.z, o0.w, o1.x, o1.y, o1.z, o1.w};
    u32x4 res;
#pragma unroll
    for (int i = 0; i < 4; ++i) {
      const unsigned o2 = pk2(ov[2 * i], ov[2 * i + 1]);
      const float a0 = bflo(o2), a1 = bfhi(o2);
      const float g0 = a0 * __builtin_amdgcn_rcpf(1.0f + __builtin_amdgcn_exp2f(-1.4426950408889634f * a0));
      const float g1 = a1 * __builtin_amdgcn_rcpf(1.0f + __builtin_amdgcn_exp2f(-1.4426950408889634f * a1));
      const unsigned g2 = pk2(g0, g1);
      const unsigned m2 = pk2(bflo(sq[i]) * bflo(g2), bfhi(sq[i]) * bfhi(g2));
      res[i] = pk2(bflo(xq[i]) + bflo(m2), bfhi(xq[i]) + bfhi(m2));
    }
    *(u32x4*)(A.out + (size_t)(mt2 * 64 + tok) * D_ + nt2 * 64 + d8 * 8) = res;
  }
}

template <int MODE>
__global__ __launch_bounds__(768, 3) void fused_kernel(Args A) {
  extern __shared__ __attribute__((aligned(16))) char smem[];
  const int tid = threadIdx.x;
  const rsrc_t ws = __builtin_amdgcn_make_buffer_rsrc(A.ws, 0, WS_END, 0x00020000);
  u32x2 epoch = {0u, 0u}, tag = {0u, 0u};
  if (MODE & PH_WAIT) {
    epoch = ld8(ws, WS_EPOCH);
    epoch.x = __builtin_amdgcn_readfirstlane(epoch.x); epoch.y = __builtin_amdgcn_readfirstlane(epoch.y);
    u64 z = (((u64)epoch.y << 32) | epoch.x) + 0x9E3779B97F4A7C15ull;
    z = (z ^ (z >> 30)) * 0xBF58476D1CE4E5B9ull; z = (z ^ (z >> 27)) * 0x94D049BB133111EBull; z ^= z >> 31;
    tag = u32x2{(unsigned)z, (unsigned)(z >> 32)};
  }
  if (__builtin_amdgcn_readfirstlane(tid) >= 512) {
    if (tid - 512 < 64) *(volatile LAS unsigned*)ldsp(L_CTL + 4u * (tid - 512)) = 0u;
    wait_lgkm();
    __builtin_amdgcn_s_barrier();
    fft_role(A, MODE, tid - 512, ws, tag, epoch);
  } else {
    gemm_role(A, MODE, tid, ws, tag);
  }
}

extern "C" void kernel_launch(void* const* d_in, const int* in_sizes, int n_in, void* d_out, int out_size, void* d_ws,
                              size_t ws_size, hipStream_t stream) {
  Args A;
  A.x = (const uint16_t*)d_in[0]; A.wr = (const uint16_t*)d_in[1]; A.wi = (const uint16_t*)d_in[2];
  A.wg = (const uint16_t*)d_in[4]; A.wu = (const uint16_t*)d_in[5]; A.wd = (const uint16_t*)d_in[6];
  A.out = (uint16_t*)d_out; A.ws = (char*)d_ws;
#define LAUNCH(M) do { (void)hipFuncSetAttribute((const void*)fused_kernel<M>, hipFuncAttributeMaxDynamicSharedMemorySize, L_TOTAL); \
    fused_kernel<M><<<256, 768, L_TOTAL, stream>>>(A); } while (0)
  LAUNCH(PH_P0);
  LAUNCH(PH_G1 | PH_P2);
  LAUNCH(PH_P3);
  LAUNCH(PH_G2);
}
```

```cpp
#include <hip/hip_runtime.h>
#include <stdint.h>

#define MULTI_LAUNCH 0
#define LTHR 20
#define PSLEEP 4
#define ROT1 0
#define HWIN 8

#define DI __device__ __forceinline__
#define LAS __attribute__((address_space(3)))
typedef __attribute__((ext_vector_type(2))) float cf;
typedef __attribute__((ext_vector_type(4))) float f32x4;
typedef __attribute__((ext_vector_type(16))) float f32x16;
typedef __attribute__((ext_vector_type(8))) short bf16x8;
typedef __attribute__((ext_vector_type(4))) short s16x4;
typedef __attribute__((ext_vector_type(2))) __bf16 bf16x2;
typedef __attribute__((ext_vector_type(4))) unsigned u32x4;
typedef __attribute__((ext_vector_type(2))) unsigned u32x2;
typedef unsigned long long u64;

constexpr int D_ = 512, DF_ = 257;
constexpr unsigned WS_EPOCH = 0;
constexpr unsigned WS_FB = 4096;
constexpr unsigned WS_FA = 131072, WS_FC = 147456, WS_FD = 163840, WS_FE = 180224;
constexpr unsigned WS_CS = 262144;
constexpr unsigned WS_STATS = 32768;
constexpr unsigned WS_W2 = 65536;
constexpr unsigned WS_X = 1u << 20;
constexpr unsigned WS_Z = 5u << 20;
constexpr unsigned WS_W = 9u << 20;
constexpr unsigned WS_S = 11u << 20;
constexpr unsigned WS_H = 13u << 20;
constexpr unsigned WS_END = 20u << 20;
constexpr unsigned L_SLOT = 40960;
constexpr unsigned L_SPARE = 122880;
constexpr unsigned L_CS = 139264;
constexpr unsigned L_FFT = 143360;
constexpr unsigned L_STAT = L_FFT + 18688;
constexpr unsigned L_ST8 = L_STAT + 1024;
constexpr unsigned L_CTL = L_ST8 + 64;
constexpr unsigned L_TOTAL = L_CTL + 704;
static_assert(L_TOTAL <= 163840, "LDS");
constexpr int EP = 292, KP = 36;
enum { C_FULL0 = 0, C_FULL1 = 1, C_PAUSE = 2, C_FSYNC = 3, C_FPUB = 4, C_GPUB = 5, C_AREADY = 6, C_CREADY = 7, C_HREADY = 8,
       C_SXREADY = 9, C_RED = 10, C_EPUB = 11, C_FREE2 = 12, C_MM = 13, C_EREADY = 14, C_FDONE = 15, C_FR0 = 16  , C_FEND = 19, C_CS0 = 20   };
enum { PH_P0 = 1, PH_G1 = 2, PH_P2 = 4, PH_P3 = 8, PH_G2 = 16, PH_WAIT = 32 };
constexpr unsigned SPIN_CAP = 1u << 14;

struct Args {
  const uint16_t *x, *wr, *wi, *wg, *wu, *wd;
  uint16_t* out;
  char* ws;
};

DI float bflo(unsigned u) { return __uint_as_float(u << 16); }
DI float bfhi(unsigned u) { return __uint_as_float(u & 0xffff0000u); }
DI unsigned pk2(float a, float b) { cf v = {a, b}; bf16x2 c = __builtin_convertvector(v, bf16x2); return __builtin_bit_cast(unsigned, c); }
DI float rbf(float a) { return bflo(pk2(a, a)); }
DI cf cis(float rev) { return cf{__builtin_amdgcn_cosf(rev), __builtin_amdgcn_sinf(rev)}; }

#define SUBM "neg_lo:[0,1] neg_hi:[0,1]"
#define SWP "op_sel:[0,1] op_sel_hi:[1,0]"
#define ADDROT_F SWP " neg_hi:[0,1]"
#define SUBROT_F SWP " neg_lo:[0,1]"
#define ADDROT_I SWP " neg_lo:[0,1]"
#define SUBROT_I SWP " neg_hi:[0,1]"
#define DFT8_BODY(AR, SR, RSUB, FAR, FSR)                                                                          \
  "v_pk_add_f32 %8, %0, %4\n\tv_pk_add_f32 %12, %0, %4 " SUBM "\n\t"                                               \
  "v_pk_add_f32 %9, %1, %5\n\tv_pk_add_f32 %13, %1, %5 " SUBM "\n\t"                                               \
  "v_pk_add_f32 %10, %2, %6\n\tv_pk_add_f32 %14, %2, %6 " SUBM "\n\t"                                              \
  "v_pk_add_f32 %11, %3, %7\n\tv_pk_add_f32 %15, %3, %7 " SUBM "\n\t"                                              \
  "v_pk_add_f32 %1, %13, %13 " AR "\n\t"                                                                           \
  "v_pk_add_f32 %0, %8, %10\n\tv_pk_add_f32 %2, %8, %10 " SUBM "\n\t"                                              \
  "v_pk_add_f32 %3, %15, %15 " RSUB "\n\t"                                                                         \
  "v_pk_add_f32 %4, %9, %11\n\tv_pk_add_f32 %6, %9, %11 " SUBM "\n\t"                                              \
  "v_pk_add_f32 %5, %12, %14 " AR "\n\tv_pk_add_f32 %7, %12, %14 " SR "\n\t"                                       \
  "v_pk_add_f32 %8, %1, %3\n\tv_pk_add_f32 %9, %1, %3 " SUBM "\n\t"                                                \
  "v_pk_add_f32 %12, %0, %4\n\tv_pk_add_f32 %13, %0, %4 " SUBM "\n\t"                                              \
  "v_pk_add_f32 %14, %2, %6 " AR "\n\tv_pk_add_f32 %15, %2, %6 " SR "\n\t"                                         \
  "v_pk_fma_f32 %10, %8, %16, %5\n\tv_pk_fma_f32 %11, %9, %16, %7 " FAR "\n\t"                                     \
  "v_pk_fma_f32 %8, %8, %16, %5 neg_lo:[1,0,0] neg_hi:[1,0,0]\n\tv_pk_fma_f32 %9, %9, %16, %7 " FSR
#define FSWP "op_sel:[1,0,0] op_sel_hi:[0,1,1]"
template <int INV> DI void dft8(cf (&v)[8]) {
  const u64 R = 0x3F3504F33F3504F3ull;
  cf t0, t1, t2, t3, t4, t5, t6, t7;
  if (!INV)
    asm(DFT8_BODY(ADDROT_F, SUBROT_F, SWP " neg_lo:[1,0] neg_hi:[1,1]", FSWP " neg_hi:[1,0,0]", FSWP " neg_lo:[1,0,0]")
        : "+v"(v[0]), "+v"(v[1]), "+v"(v[2]), "+v"(v[3]), "+v"(v[4]), "+v"(v[5]), "+v"(v[6]), "+v"(v[7]),
          "=&v"(t0), "=&v"(t1), "=&v"(t2), "=&v"(t3), "=&v"(t4), "=&v"(t5), "=&v"(t6), "=&v"(t7) : "s"(R));
  else
    asm(DFT8_BODY(ADDROT_I, SUBROT_I, SWP " neg_lo:[1,1] neg_hi:[1,0]", FSWP " neg_lo:[1,0,0]", FSWP " neg_hi:[1,0,0]")
        : "+v"(v[0]), "+v"(v[1]), "+v"(v[2]), "+v"(v[3]), "+v"(v[4]), "+v"(v[5]), "+v"(v[6]), "+v"(v[7]),
          "=&v"(t0), "=&v"(t1), "=&v"(t2), "=&v"(t3), "=&v"(t4), "=&v"(t5), "=&v"(t6), "=&v"(t7) : "s"(R));
  v[0] = t4; v[1] = t2; v[2] = t6; v[3] = t3; v[4] = t5; v[5] = t0; v[6] = t7; v[7] = t1;
}
#define DFT4X2_BODY(AR, SR)                                                                                        \
  "v_pk_add_f32 %8, %0, %2\n\tv_pk_add_f32 %9, %0, %2 " SUBM "\n\t"                                                \
  "v_pk_add_f32 %10, %1, %3\n\tv_pk_add_f32 %11, %1, %3 " SUBM "\n\t"                                              \
  "v_pk_add_f32 %12, %4, %6\n\tv_pk_add_f32 %13, %4, %6 " SUBM "\n\t"                                              \
  "v_pk_add_f32 %14, %5, %7\n\tv_pk_add_f32 %15, %5, %7 " SUBM "\n\t"                                              \
  "v_pk_add_f32 %0, %8, %10\n\tv_pk_add_f32 %2, %8, %10 " SUBM "\n\t"                                              \
  "v_pk_add_f32 %1, %9, %11 " AR "\n\tv_pk_add_f32 %3, %9, %11 " SR "\n\t"                                         \
  "v_pk_add_f32 %4, %12, %14\n\tv_pk_add_f32 %6, %12, %14 " SUBM "\n\t"                                            \
  "v_pk_add_f32 %5, %13, %15 " AR "\n\tv_pk_add_f32 %7, %13, %15 " SR
template <int INV> DI void dft4x2(cf (&v)[8]) {
  cf t0, t1, t2, t3, t4, t5, t6, t7;
  if (!INV)
    asm(DFT4X2_BODY(ADDROT_F, SUBROT_F)
        : "+v"(v[0]), "+v"(v[1]), "+v"(v[2]), "+v"(v[3]), "+v"(v[4]), "+v"(v[5]), "+v"(v[6]), "+v"(v[7]),
          "=&v"(t0), "=&v"(t1), "=&v"(t2), "=&v"(t3), "=&v"(t4), "=&v"(t5), "=&v"(t6), "=&v"(t7));
  else
    asm(DFT4X2_BODY(ADDROT_I, SUBROT_I)
        : "+v"(v[0]), "+v"(v[1]), "+v"(v[2]), "+v"(v[3]), "+v"(v[4]), "+v"(v[5]), "+v"(v[6]), "+v"(v[7]),
          "=&v"(t0), "=&v"(t1), "=&v"(t2), "=&v"(t3), "=&v"(t4), "=&v"(t5), "=&v"(t6), "=&v"(t7));
}
#define TWM(t, a, w) "v_pk_mul_f32 %" #t ", %" #a ", %" #w " op_sel_hi:[1,0]\n\t"
#define TWF(t, a, w, N) "v_pk_fma_f32 %" #t ", %" #a ", %" #w ", %" #t " op_sel:[1,1,0] op_sel_hi:[0,1,1] " N "\n\t"
#define TW7_BODY(N)                                                                                                \
  "s_nop 0\n\t" TWM(0, 7, 14) TWM(1, 8, 15) TWM(2, 9, 16) TWM(3, 10, 17) TWM(4, 11, 18) TWM(5, 12, 19) TWM(6, 13, 20) \
  TWF(0, 7, 14, N) TWF(1, 8, 15, N) TWF(2, 9, 16, N) TWF(3, 10, 17, N) TWF(4, 11, 18, N) TWF(5, 12, 19, N) TWF(6, 13, 20, N)
template <int INV> DI void tw7(cf (&v)[8], const cf (&w)[7]) {
  cf t0, t1, t2, t3, t4, t5, t6;
  if (!INV)
    asm(TW7_BODY("neg_lo:[0,1,0]")
        : "=&v"(t0), "=&v"(t1), "=&v"(t2), "=&v"(t3), "=&v"(t4), "=&v"(t5), "=&v"(t6)
        : "v"(v[1]), "v"(v[2]), "v"(v[3]), "v"(v[4]), "v"(v[5]), "v"(v[6]), "v"(v[7]),
          "v"(w[0]), "v"(w[1]), "v"(w[2]), "v"(w[3]), "v"(w[4]), "v"(w[5]), "v"(w[6]));
  else
    asm(TW7_BODY("neg_hi:[0,1,0]")
        : "=&v"(t0), "=&v"(t1), "=&v"(t2), "=&v"(t3), "=&v"(t4), "=&v"(t5), "=&v"(t6)
        : "v"(v[1]), "v"(v[2]), "v"(v[3]), "v"(v[4]), "v"(v[5]), "v"(v[6]), "v"(v[7]),
          "v"(w[0]), "v"(w[1]), "v"(w[2]), "v"(w[3]), "v"(w[4]), "v"(w[5]), "v"(w[6]));
  v[1] = t0; v[2] = t1; v[3] = t2; v[4] = t3; v[5] = t4; v[6] = t5; v[7] = t6;
}
#define FL1(r, p, a) "v_pk_mul_f32 %" #r ", %" #p ", %" #a " op_sel_hi:[1,0]\n\t"
#define FL2(r, p, a) "v_pk_fma_f32 %" #r ", %" #p ", %" #a ", %" #r " op_sel:[1,1,0] op_sel_hi:[0,1,1] neg_lo:[0,1,0]\n\t"
#define FL3(r, b, q) "v_pk_fma_f32 %" #r ", %" #b ", %" #q ", %" #r " op_sel_hi:[1,0,1]\n\t"
#define FL4(r, b, q) "v_pk_fma_f32 %" #r ", %" #b ", %" #q ", %" #r " op_sel:[1,1,0] op_sel_hi:[0,1,1] neg_hi:[0,1,0]\n\t"
DI void filt4(cf* v, const cf* q, const cf* al, const cf* be) {
  cf r0, r1, r2, r3;
  asm(FL1(0, 4, 12) FL1(1, 5, 13) FL1(2, 6, 14) FL1(3, 7, 15) FL2(0, 4, 12) FL2(1, 5, 13) FL2(2, 6, 14) FL2(3, 7, 15)
      FL3(0, 16, 8) FL3(1, 17, 9) FL3(2, 18, 10) FL3(3, 19, 11) FL4(0, 16, 8) FL4(1, 17, 9) FL4(2, 18, 10) FL4(3, 19, 11)
      : "=&v"(r0), "=&v"(r1), "=&v"(r2), "=&v"(r3)
      : "v"(v[0]), "v"(v[1]), "v"(v[2]), "v"(v[3]), "v"(q[0]), "v"(q[1]), "v"(q[2]), "v"(q[3]),
        "v"(al[0]), "v"(al[1]), "v"(al[2]), "v"(al[3]), "v"(be[0]), "v"(be[1]), "v"(be[2]), "v"(be[3]));
  v[0] = r0; v[1] = r1; v[2] = r2; v[3] = r3;
}
#define CNDD(d, s0, s1, QP) "v_cndmask_b32_dpp %" #d ", %" #s0 ", %" #s1 ", vcc quad_perm:" QP " row_mask:0xf bank_mask:0xf\n\t"
#define SW4_BODY(QP)                                                                                               \
  "s_mov_b64 vcc, %16\n\ts_nop 0\n\t" CNDD(4, 8, 12, QP) CNDD(5, 9, 13, QP) CNDD(6, 10, 14, QP) CNDD(7, 11, 15, QP)   \
  "s_not_b64 vcc, vcc\n\t" CNDD(0, 12, 8, QP) CNDD(1, 13, 9, QP) CNDD(2, 14, 10, QP) CNDD(3, 15, 11, QP)
template <int X> DI void sw4(float& a0, float& a1, float& a2, float& a3, float& b0, float& b1, float& b2, float& b3) {
  float c0, c1, c2, c3, d0, d1, d2, d3;
  if (X == 1) {
    const u64 m = 0xAAAAAAAAAAAAAAAAull;
    asm(SW4_BODY("[1,0,3,2]") : "=&v"(c0), "=&v"(c1), "=&v"(c2), "=&v"(c3), "=&v"(d0), "=&v"(d1), "=&v"(d2), "=&v"(d3)
        : "v"(a0), "v"(a1), "v"(a2), "v"(a3), "v"(b0), "v"(b1), "v"(b2), "v"(b3), "s"(m) : "vcc", "scc");
  } else {
    const u64 m = 0xCCCCCCCCCCCCCCCCull;
    asm(SW4_BODY("[2,3,0,1]") : "=&v"(c0), "=&v"(c1), "=&v"(c2), "=&v"(c3), "=&v"(d0), "=&v"(d1), "=&v"(d2), "=&v"(d3)
        : "v"(a0), "v"(a1), "v"(a2), "v"(a3), "v"(b0), "v"(b1), "v"(b2), "v"(b3), "s"(m) : "vcc", "scc");
  }
  a0 = c0; a1 = c1; a2 = c2; a3 = c3; b0 = d0; b1 = d1; b2 = d2; b3 = d3;
}
template <int X> DI void swc(cf& a0, cf& a1, cf& b0, cf& b1) {
  float p0 = a0.x, p1 = a0.y, p2 = a1.x, p3 = a1.y, q0 = b0.x, q1 = b0.y, q2 = b1.x, q3 = b1.y;
  sw4<X>(p0, p1, p2, p3, q0, q1, q2, q3);
  a0 = cf{p0, p1}; a1 = cf{p2, p3}; b0 = cf{q0, q1}; b1 = cf{q2, q3};
}
DI void quadT2(cf (&v)[8]) {
  swc<1>(v[0], v[2], v[1], v[3]); swc<1>(v[4], v[6], v[5], v[7]);
  swc<2>(v[0], v[1], v[2], v[3]); swc<2>(v[4], v[5], v[6], v[7]);
}
template <int CTRL> DI float dppf(float v) { return __int_as_float(__builtin_amdgcn_update_dpp(0, __float_as_int(v), CTRL, 0xf, 0xf, true)); }
template <int CTRL> DI unsigned dppu(unsigned v) { return (unsigned)__builtin_amdgcn_update_dpp(0, (int)v, CTRL, 0xf, 0xf, true); }
DI float red32(float v) {
  v += dppf<0xB1>(v); v += dppf<0x4E>(v); v += dppf<0x141>(v); v += dppf<0x140>(v);
  auto r = __builtin_amdgcn_permlane16_swap(__float_as_uint(v), __float_as_uint(v), false, false);
  return __uint_as_float(r[0]) + __uint_as_float(r[1]);
}
DI float sadd(float a, float b) { float r; asm("v_add_f32 %0, %1, %2" : "=v"(r) : "v"(a), "v"(b)); return r; }
DI int opaque(int v) { asm volatile("" : "+v"(v)); return v; }

#define STAMP(i)
#define STAMPL(i)
#define STAMPH(i)
#define STAMPC(i)
#define STAMP_INIT(lo, n)
#define STAMP_DUMP(lo, n)
DI LAS char* ldsp(unsigned off) { return (LAS char*)(uintptr_t)off; }
DI cf lds_ld(unsigned off) { return *(LAS cf*)ldsp(off); }
DI void lds_st(unsigned off, cf v) { *(LAS cf*)ldsp(off) = v; }
DI void wait_lgkm() { asm volatile("s_waitcnt lgkmcnt(0)" ::: "memory"); }
DI void fence_c() { asm volatile("" ::: "memory"); }
template <int N> DI void wait_vm() { asm volatile("s_waitcnt vmcnt(%0)" ::"i"(N) : "memory"); }
DI void ctl_add(int idx) {
  const unsigned a = L_CTL + 4 * idx, one = 1u;
  asm volatile("s_waitcnt lgkmcnt(0)\n\ts_mov_b64 exec, 1\n\tds_add_u32 %0, %1\n\ts_mov_b64 exec, -1" ::"v"(a), "v"(one) : "memory");
}
DI unsigned ctl_add_rtn(int idx) {
  const unsigned a = L_CTL + 4 * idx, one = 1u;
  unsigned r;
  asm volatile("s_waitcnt lgkmcnt(0)\n\ts_mov_b64 exec, 1\n\tds_add_rtn_u32 %0, %1, %2\n\ts_mov_b64 exec, -1\n\ts_waitcnt lgkmcnt(0)"
               : "=&v"(r) : "v"(a), "v"(one) : "memory");
  return __builtin_amdgcn_readfirstlane(r);
}
DI void ctl_set(int idx, unsigned v) {
  const unsigned a = L_CTL + 4 * idx;
  asm volatile("s_waitcnt lgkmcnt(0)\n\ts_mov_b64 exec, 1\n\tds_write_b32 %0, %1\n\ts_mov_b64 exec, -1" ::"v"(a), "v"(v) : "memory");
}
DI unsigned ctl_get(int idx) { return __builtin_amdgcn_readfirstlane(*(volatile LAS unsigned*)ldsp(L_CTL + 4 * idx)); }
template <int SLEEP> DI void ctl_wait(int idx, unsigned target) {
  const unsigned a = L_CTL + 4 * idx;
  unsigned v, sv, cnt;
  asm volatile("s_mov_b32 %2, 0x20000\n"
               "1:\n\tds_read_b32 %0, %3\n\ts_waitcnt lgkmcnt(0)\n\tv_readfirstlane_b32 %1, %0\n\ts_cmp_ge_u32 %1, %4\n\ts_cbranch_scc1 2f\n\t"
               "s_sleep %5\n\ts_sub_u32 %2, %2, 1\n\ts_cbranch_scc0 1b\n2:"
               : "=&v"(v), "=&s"(sv), "=&s"(cnt) : "v"(a), "s"(__builtin_amdgcn_readfirstlane(target)), "i"(SLEEP) : "memory", "scc");
}
template <int SLEEP> DI void full_wait(unsigned target) {
  const unsigned a = L_CTL;
  unsigned v0, v1, v2, sv, cnt;
  asm volatile("s_mov_b32 %4, 0x20000\n"
               "1:\n\tds_read_b32 %0, %5\n\tds_read_b32 %1, %5 offset:4\n\tds_read_b32 %2, %5 offset:8\n\ts_waitcnt lgkmcnt(0)\n\t"
               "v_min3_u32 %0, %0, %1, %2\n\ts_nop 0\n\tv_readfirstlane_b32 %3, %0\n\ts_cmp_ge_u32 %3, %6\n\ts_cbranch_scc1 2f\n\t"
               "s_sleep %7\n\ts_sub_u32 %4, %4, 1\n\ts_cbranch_scc0 1b\n2:"
               : "=&v"(v0), "=&v"(v1), "=&v"(v2), "=&s"(sv), "=&s"(cnt) : "v"(a), "s"(__builtin_amdgcn_readfirstlane(target)), "i"(SLEEP) : "memory", "scc");
}
DI void fft_end(unsigned target) { if (ctl_add_rtn(C_FEND) + 1 == target) ctl_set(C_PAUSE, ~0u); }
DI void glds(unsigned voff, const void* sbase, unsigned ldsdst) {
  unsigned keep;
  asm volatile("s_mov_b32 %0, m0\n\ts_mov_b32 m0, %3\n\ts_nop 0\n\tglobal_load_lds_dwordx4 %1, %2\n\ts_mov_b32 m0, %0"
               : "=&s"(keep) : "v"(voff), "s"(sbase), "s"(ldsdst) : "memory");
}
DI void glds_m(unsigned voff, const void* sbase, unsigned ldsdst, u64 mask) {
  unsigned keep;
  asm volatile("s_mov_b32 %0, m0\n\ts_mov_b32 m0, %3\n\ts_mov_b64 exec, %4\n\tglobal_load_lds_dwordx4 %1, %2\n\ts_mov_b64 exec, -1\n\ts_mov_b32 m0, %0"
               : "=&s"(keep) : "v"(voff), "s"(sbase), "s"(ldsdst), "s"(mask) : "memory");
}
typedef __amdgpu_buffer_rsrc_t rsrc_t;
DI u32x4 ld16(rsrc_t r, unsigned off) { return __builtin_amdgcn_raw_buffer_load_b128(r, off, 0, 16); }
DI u32x2 ld8(rsrc_t r, unsigned off) { return __builtin_amdgcn_raw_buffer_load_b64(r, off, 0, 16); }
DI void st16(rsrc_t r, unsigned off, u32x4 v) { __builtin_amdgcn_raw_buffer_store_b128(v, r, off, 0, 16); }
DI void st8(rsrc_t r, unsigned off, u32x2 v) { __builtin_amdgcn_raw_buffer_store_b64(v, r, off, 0, 16); }
DI void st4(rsrc_t r, unsigned off, unsigned v) { __builtin_amdgcn_raw_buffer_store_b32(v, r, off, 0, 16); }
DI u32x4 cc2(cf a, cf b) { return u32x4{__float_as_uint(a.x), __float_as_uint(a.y), __float_as_uint(b.x), __float_as_uint(b.y)}; }
DI cf u2c(unsigned a, unsigned b) { return cf{__uint_as_float(a), __uint_as_float(b)}; }

DI u32x2 make_tag(u32x2 epoch) {
  u64 z = (((u64)__builtin_amdgcn_readfirstlane(epoch.y) << 32) | (unsigned)__builtin_amdgcn_readfirstlane(epoch.x)) + 0x9E3779B97F4A7C15ull;
  z = (z ^ (z >> 30)) * 0xBF58476D1CE4E5B9ull; z = (z ^ (z >> 27)) * 0x94D049BB133111EBull; z ^= z >> 31;
  return u32x2{(unsigned)z, (unsigned)(z >> 32)};
}
template <int NREP> DI void publish(rsrc_t ws, int cidx, unsigned target, unsigned flag_off, u32x2 tag, int lane) {
  wait_vm<0>();
  const unsigned r = ctl_add_rtn(cidx);
  if (r + 1 == target && lane < NREP) st8(ws, flag_off + 2048u * lane, tag);
}
DI void poll_flags(rsrc_t ws, unsigned flag_off, int nflags, u32x2 tag, int lane) {
  const unsigned o = flag_off + 32u * lane;
  bool done = 4 * lane >= nflags;
  for (unsigned spins = 0;; ++spins) {
    fence_c();
    if (!done) {
      const u32x4 a = ld16(ws, o), b = ld16(ws, o + 16);
      done = a.x == tag.x && a.y == tag.y && a.z == tag.x && a.w == tag.y && b.x == tag.x && b.y == tag.y && b.z == tag.x && b.w == tag.y;
    }
    if (__all(done)) break;
    if (spins > SPIN_CAP) break;
    __builtin_amdgcn_s_sleep(PSLEEP);
  }
  fence_c();
}
DI void twiddles(cf (&w)[7], int pos, float scale) {
#pragma unroll
  for (int i = 1; i < 8; ++i) { w[i - 1] = cis(-(float)(pos * i) * scale); asm volatile("" : "+v"(w[i - 1])); }
}

struct __attribute__((packed, aligned(2))) U8 { unsigned a, b; };
DI float smul(float a, float b) { float r; asm("v_mul_f32 %0, %1, %2" : "=v"(r) : "v"(a), "v"(b)); return r; }
DI float ssub(float a, float b) { float r; asm("v_sub_f32 %0, %1, %2" : "=v"(r) : "v"(a), "v"(b)); return r; }
DI float sfma(float a, float b, float c) { float r; asm("v_fma_f32 %0, %1, %2, %3" : "=v"(r) : "v"(a), "v"(b), "v"(c)); return r; }
DI void lane_swap(float& a, float& b, int lane) {
  const int sa = __builtin_amdgcn_readlane(__float_as_int(a), 59), sb = __builtin_amdgcn_readlane(__float_as_int(b), 63);
  a = lane == 59 ? __int_as_float(sb) : a;
  b = lane == 63 ? __int_as_float(sa) : b;
}
DI void special_swap(cf (&v)[8], int lane) {
#pragma unroll
  for (int r = 1; r < 4; ++r) {
    float ax = v[8 - r].x, ay = v[8 - r].y, bx = v[r].x, by = v[r].y;
    lane_swap(ax, bx, lane); lane_swap(ay, by, lane);
    v[8 - r] = cf{ax, ay}; v[r] = cf{bx, by};
  }
}
DI void twiddles_c(cf (&w)[7], int b, bool side) {
#pragma unroll
  for (int i = 1; i < 8; ++i) { w[i - 1] = cis(-(float)(b * (side ? 8 - i : i)) * (1.0f / 2048.0f)); asm volatile("" : "+v"(w[i - 1])); }
}

template <int mode> DI void fft_role(const Args& A, const int t_, rsrc_t ws, u32x2 epoch) {
  u32x2 tag = {0u, 0u};
  const int t = t_;
  const int lane = t & 63, w = __builtin_amdgcn_readfirstlane(t >> 6), bid = blockIdx.x;
  constexpr bool WAIT = mode & PH_WAIT;
  const int hf = lane >> 5;
  const int l5 = lane & 31, k1l = (lane >> 2) & 7, llo = lane & 3, kb = k1l + 8 * llo;
  unsigned fs = 0;
  auto fsync = [&]() { fs += 4; ctl_add(C_FSYNC); ctl_wait<0>(C_FSYNC, fs); };
  const int k0 = 4 * (t >> 3) + 1 + (t & 3);
  const int kt = (t & 4) ? (t == 255 ? 0 : 256 - k0) : k0;
  cf tw2[7];
  cf v[8];
  STAMP(16);
  if (mode & PH_P0) {
    const int ra = hf ? (w ? 8 - w : 4) : w, rn = (8 - ra) & 7;
    unsigned raw[8];
    const unsigned* xr = (const unsigned*)(A.x + (size_t)(bid + 256 * ra) * D_);
#pragma unroll
    for (int i = 0; i < 8; ++i) raw[i] = xr[l5 + 32 * i];
    cf twA[7], twc[7];
    twiddles(twA, l5, 1.0f / 256.0f);
    twiddles(tw2, llo, 1.0f / 32.0f);
    twiddles_c(twc, bid, t & 4);
    const unsigned aW1 = L_FFT + 8u * (ra * EP + l5), aR1 = L_FFT + 8u * (ra * EP + k1l * KP + llo);
    const unsigned aLo = L_FFT + 8u * (ra * EP + kb), aHi = L_FFT + 8u * (rn * EP + kb);
    const unsigned a0 = kb ? aLo : aHi, a4 = kb ? aHi : aLo;
    __builtin_amdgcn_sched_barrier(0);
    cf s2 = {0.f, 0.f};
#pragma unroll
    for (int i = 0; i < 8; ++i) { v[i] = cf{bflo(raw[i]), bfhi(raw[i])}; s2 += v[i]; }
    const float mean = red32(s2.x + s2.y) * (1.0f / D_);
    STAMP(17);
    cf q2 = {0.f, 0.f};
#pragma unroll
    for (int i = 0; i < 8; ++i) { v[i] = v[i] - mean; q2 += v[i] * v[i]; }
    const float rinv = __builtin_amdgcn_rsqf(red32(q2.x + q2.y) * (1.0f / D_) + 1e-5f);
#pragma unroll
    for (int i = 0; i < 8; ++i) v[i] = v[i] * rinv;
    lds_st(L_ST8 + 8u * ra, cf{mean, rinv});
    dft8<0>(v); tw7<0>(v, twA);
#pragma unroll
    for (int i = 0; i < 8; ++i) lds_st(aW1 + 8u * KP * i, v[i]);
    fence_c();
#pragma unroll
    for (int i = 0; i < 8; ++i) v[i] = lds_ld(aR1 + 32u * i);
    dft8<0>(v); tw7<0>(v, tw2);
    quadT2(v);
    dft4x2<0>(v);
    fence_c();
#pragma unroll
    for (int i = 0; i < 8; ++i) { const int n = (i >> 2) + 2 * (i & 3); lds_st((n == 0 ? a0 : n == 4 ? a4 : n < 4 ? aLo : aHi) + 256u * n, v[i]); }
    fsync();
#pragma unroll
    for (int i = 0; i < 8; ++i) v[i] = lds_ld(L_FFT + 8u * (i * EP) + 8u * kt);
    if (t < 8) { const cf sv = lds_ld(L_ST8 + 8u * t); st8(ws, WS_STATS + 64u * bid + 8u * t, u32x2{__float_as_uint(sv.x), __float_as_uint(sv.y)}); }
    dft8<0>(v); tw7<0>(v, twc);
    if (w == 3) special_swap(v, lane);
    const unsigned o = WS_X + (unsigned)(t >> 3) * 131072u + (unsigned)bid * 64u + 8u * (t & 7);
#pragma unroll
    for (int i = 0; i < 8; ++i) st8(ws, o + 16384u * i, u32x2{__float_as_uint(v[i].x), __float_as_uint(v[i].y)});
    if (WAIT) { fft_end(4); tag = make_tag(epoch); }
    STAMP(18);
    if (WAIT) publish<8>(ws, C_FPUB, 4, WS_FA + 8u * bid, tag, lane);
    STAMP(19);
  } else {
    twiddles(tw2, llo, 1.0f / 32.0f);
  }
  if (mode & PH_P2) {
    const int t = opaque(t_), lane = t & 63, hf = lane >> 5;
    const int k1l = (lane >> 2) & 7, llo = lane & 3, kb = k1l + 8 * llo;
    const int c = bid & 7, q = bid >> 3, eB = t & 7, p5 = t >> 3;
    const bool spq = q == 31 && (c >= 4 || c == 0);
    const int e = w + 4 * hf;
    const bool selfp = q == 31 && w == 3 && (c & 3) == 0;
    const bool sym = q == 31 && w == 3 && (c >= 5 || ((c & 3) == 0 && hf));
    const int ep = selfp ? e : e ^ 4;
    const int kcol = sym ? 0 : (hf ? 255 - 4 * q - w : 4 * q + 1 + w);
    const int dneg = c ? 255 : 256;
    const int kbp = (dneg - kb) & 31;
    const bool spec = (dneg - kb) == 256;
    cf twB[7];
    twiddles(twB, p5, 1.0f / 256.0f);
    constexpr float SC = 0.5f / (2048.0f * 256.0f);
    const cf cs = cis((float)kcol * (1.0f / 512.0f));
    const float ca = SC - SC * cs.y, cb = SC + SC * cs.y, cg = SC * cs.x;
    const unsigned aB = L_FFT + 8u * (eB * EP + p5), aR = L_FFT + 8u * (e * EP + k1l * KP + llo), aK = L_FFT + 8u * (e * EP + kb);
    const unsigned aQ = L_FFT + 8u * (ep * EP + kbp);
    if (WAIT) ctl_wait<1>(C_EREADY, 1u);
    cf al[8], be[8];
    {
      const unsigned wo = WS_W + (unsigned)bid * 8192u + (unsigned)e * 1024u + 32u * kb, wpo = WS_W + (unsigned)bid * 8192u + (unsigned)ep * 1024u + 32u * kbp;
      const u32x4 wa0 = ld16(ws, wo), wa1 = ld16(ws, wo + 16), wb0 = ld16(ws, wpo), wb1 = ld16(ws, wpo + 16);
      const unsigned w1[8] = {wa0.x, wa0.y, wa0.z, wa0.w, wa1.x, wa1.y, wa1.z, wa1.w};
      const unsigned wp[8] = {wb0.x, wb0.y, wb0.z, wb0.w, wb1.x, wb1.y, wb1.z, wb1.w};
      float W1x[8], W1y[8], W2x[8], W2y[8];
#pragma unroll
      for (int i = 0; i < 8; ++i) {
        const int n = (i >> 2) + 2 * (i & 3);
        const unsigned u1 = w1[n], u2 = spec ? wp[(8 - n) & 7] : wp[7 - n];
        W1x[i] = bflo(u1); W1y[i] = bfhi(u1); W2x[i] = bflo(u2); W2y[i] = bfhi(u2);
      }
      if (spq && w == 3) {
        const unsigned o1 = WS_W2 + (unsigned)(2 * c + hf) * 1024u + 32u * kb, o2 = WS_W2 + (unsigned)(2 * c + (ep >> 2)) * 1024u + 32u * kbp;
        const u32x4 a0 = ld16(ws, o1), a1 = ld16(ws, o1 + 16), b0 = ld16(ws, o2), b1 = ld16(ws, o2 + 16);
        const unsigned y1[8] = {a0.x, a0.y, a0.z, a0.w, a1.x, a1.y, a1.z, a1.w};
        const unsigned yp[8] = {b0.x, b0.y, b0.z, b0.w, b1.x, b1.y, b1.z, b1.w};
#pragma unroll
        for (int i = 0; i < 8; ++i) {
          const int n = (i >> 2) + 2 * (i & 3);
          const unsigned u1 = y1[n], u2 = spec ? yp[(8 - n) & 7] : yp[7 - n];
          if (sym) {
            W1x[i] = smul(sadd(W1x[i], W2x[i]), 0.5f); W1y[i] = smul(ssub(W1y[i], W2y[i]), 0.5f);
            W2x[i] = smul(sadd(bflo(u1), bflo(u2)), 0.5f); W2y[i] = smul(ssub(bfhi(u2), bfhi(u1)), 0.5f);
          }
        }
      }
      const float ncb = -cb, ncg = -cg;
#pragma unroll
      for (int i = 0; i < 8; ++i) {
        al[i] = cf{sfma(W1x[i], ca, smul(W2x[i], cb)), sfma(W1y[i], ca, smul(W2y[i], ncb))};
        be[i] = cf{smul(sadd(W2y[i], W1y[i]), ncg), smul(ssub(W1x[i], W2x[i]), cg)};
        asm volatile("" : "+v"(al[i]), "+v"(be[i]));
      }
    }
    if (WAIT) ctl_wait<1>(C_AREADY, 1u);
    STAMP(20);
    const unsigned xo = WS_X + (unsigned)bid * 16384u + 8u * t;
#pragma unroll
    for (int i = 0; i < 8; ++i) { const u32x2 r = ld8(ws, xo + 2048u * i); v[i] = u2c(r.x, r.y); }
    dft8<0>(v); tw7<0>(v, twB);
    STAMP(21);
#pragma unroll
    for (int i = 0; i < 8; ++i) lds_st(aB + 8u * KP * i, v[i]);
    fsync();
#pragma unroll
    for (int i = 0; i < 8; ++i) v[i] = lds_ld(aR + 32u * i);
    dft8<0>(v); tw7<0>(v, tw2);
    quadT2(v);
    dft4x2<0>(v);
    fence_c();
#pragma unroll
    for (int i = 0; i < 8; ++i) lds_st(aK + 256u * ((i >> 2) + 2 * (i & 3)), v[i]);
    fence_c();
    cf qv[8];
    {
      const unsigned aQs = aQ + (spec ? 256u : 0u);
#pragma unroll
      for (int i = 0; i < 8; ++i) {
        const int n = (i >> 2) + 2 * (i & 3);
        if (n == 0) qv[i] = lds_ld(spec ? aQ : aQ + 256u * 7);
        else qv[i] = lds_ld(aQs + 256u * (7 - n));
      }
    }
    filt4(v, qv, al, be); filt4(v + 4, qv + 4, al + 4, be + 4);
    dft4x2<1>(v);
    quadT2(v);
    tw7<1>(v, tw2); dft8<1>(v);
    fence_c();
#pragma unroll
    for (int i = 0; i < 8; ++i) lds_st(aR + 32u * i, v[i]);
    fsync();
#pragma unroll
    for (int i = 0; i < 8; ++i) v[i] = lds_ld(aB + 8u * KP * i);
    tw7<1>(v, twB); dft8<1>(v);
    const unsigned zo = WS_Z + (unsigned)p5 * 16384u + (unsigned)c * 2048u + (unsigned)q * 64u + 8u * eB;
#pragma unroll
    for (int i = 0; i < 8; ++i) st8(ws, zo + 524288u * i, u32x2{__float_as_uint(v[i].x), __float_as_uint(v[i].y)});
    if (WAIT) fft_end(8);
    STAMP(22);
    if (WAIT) publish<8>(ws, C_FPUB, 8, WS_FC + 8u * bid, tag, lane);
    STAMP(23);
  }
  if (mode & PH_P3) {
    const int t = opaque(t_), lane = t & 63, hq = lane >> 5;
    const int l5 = lane & 31, k1l = (lane >> 2) & 7, llo = lane & 3, kb = k1l + 8 * llo;
    const int k0 = 4 * (t >> 3) + 1 + (t & 3);
    const int kt = (t & 4) ? (t == 255 ? 0 : 256 - k0) : k0;
    cf twA[7], twc[7];
    twiddles(twA, l5, 1.0f / 256.0f);
    twiddles_c(twc, bid, t & 4);
    const int ra = hq ? (w ? 8 - w : 4) : w, rn = (8 - ra) & 7;
    const unsigned aW1 = L_FFT + 8u * (ra * EP + l5), aR1 = L_FFT + 8u * (ra * EP + k1l * KP + llo);
    const unsigned aLo = L_FFT + 8u * (ra * EP + kb), aHi = L_FFT + 8u * (rn * EP + kb);
    const unsigned a0 = kb ? aLo : aHi, a4 = kb ? aHi : aLo;
    if (WAIT) ctl_wait<1>(C_CREADY, 1u);
    STAMP(24);
    const unsigned o = WS_Z + (unsigned)bid * 16384u + 8u * t;
#pragma unroll
    for (int i = 0; i < 8; ++i) { const u32x2 r = ld8(ws, o + 2048u * i); v[i] = u2c(r.x, r.y); }
    if (w == 3) special_swap(v, lane);
    tw7<1>(v, twc); dft8<1>(v);
    STAMP(25);
#pragma unroll
    for (int i = 0; i < 8; ++i) lds_st(L_FFT + 8u * (i * EP) + 8u * kt, v[i]);
    fsync();
#pragma unroll
    for (int i = 0; i < 8; ++i) { const int n = (i >> 2) + 2 * (i & 3); v[i] = lds_ld((n == 0 ? a0 : n == 4 ? a4 : n < 4 ? aLo : aHi) + 256u * n); }
    dft4x2<1>(v);
    quadT2(v);
    tw7<1>(v, tw2); dft8<1>(v);
    fence_c();
#pragma unroll
    for (int i = 0; i < 8; ++i) lds_st(aR1 + 32u * i, v[i]);
    fence_c();
#pragma unroll
    for (int i = 0; i < 8; ++i) v[i] = lds_ld(aW1 + 8u * KP * i);
    ctl_add(C_FDONE);
    tw7<1>(v, twA); dft8<1>(v);
    const unsigned so = WS_S + (unsigned)(bid + 256 * ra) * 1024u + 4u * l5;
#pragma unroll
    for (int i = 0; i < 8; ++i) st4(ws, so + 128u * i, pk2(v[i].x, v[i].y));
    if (WAIT) fft_end(12);
    STAMP(26);
    if (WAIT) publish<8>(ws, C_FPUB, 12, WS_FD + 8u * bid, tag, lane);
    STAMP(27);
  }
}

struct Tile { int mt, nt, np, start, mt2, nt2, rot1, rot2; };
DI Tile tile_of(int bid) {
  Tile T;
  T.mt = (bid & 1) * 8 + ((bid >> 3) & 7); T.nt = ((bid >> 1) & 3) * 4 + (bid >> 6);
  T.np = ((T.nt & 3) == 3 || T.nt == 14) ? 2 : 3;
  T.start = 3 * T.nt - (T.nt >> 2) - (T.nt == 15 ? 1 : 0);
  T.mt2 = (bid & 7) * 4 + ((bid >> 3) & 3); T.nt2 = bid >> 5;
  T.rot1 = ROT1 ? (T.mt + 2 * T.nt) & 7 : 0;
  T.rot2 = (11 * T.nt2 + 22 * (T.mt2 & 3)) % 86;
  return T;
}
__host__ __device__ constexpr unsigned wd_addr(int u) {
  return u >= 68 ? wd_addr(u - 68) : (u < 8 ? L_SPARE + 2048u * u : u < 28 ? 81920u + 2048u * (u - 8) : u < 48 ? 2048u * (u - 28) : 40960u + 2048u * (u - 48));
}
DI bf16x8 tr2(unsigned a0, unsigned a1) {
  const s16x4 lo = __builtin_amdgcn_ds_read_tr16_b64_v4i16((LAS s16x4*)ldsp(a0));
  const s16x4 hi = __builtin_amdgcn_ds_read_tr16_b64_v4i16((LAS s16x4*)ldsp(a1));
  return __builtin_shufflevector(lo, hi, 0, 1, 2, 3, 4, 5, 6, 7);
}
DI float dot1(unsigned u, float acc) {
  const unsigned o = 0x3F803F80u;
  return __builtin_amdgcn_fdot2_f32_bf16(__builtin_bit_cast(bf16x2, u), __builtin_bit_cast(bf16x2, o), acc, false);
}
DI float dot_ones(bf16x8 f, float acc) {
  const u32x4 u = __builtin_bit_cast(u32x4, f);
  const unsigned a = u.x, b = u.y, c = u.z, d = u.w;
  return dot1(d, dot1(c, dot1(b, dot1(a, acc))));
}
#define MFMA(a, b, c) __builtin_amdgcn_mfma_f32_32x32x16_bf16((a), (b), (c), 0, 0, 0)
DI unsigned wfrag_off(int lane, int fg) {
  const int g4 = lane >> 4, q4 = (lane >> 2) & 3, pp = lane & 3, mat = g4 & 1;
  return 16384u + (unsigned)mat * 12288u + (unsigned)(2 * (g4 >> 1)) * 768u + (unsigned)q4 * 64u + (unsigned)(fg ^ mat) * 32u + 8u * pp;
}

template <int mode> DI void load_role(const Args& A, int l, int lane) {
  const Tile T = tile_of(blockIdx.x);
  const int g4 = lane >> 4, q4 = (lane >> 2) & 3, pp = lane & 3;
  const unsigned wdv = (unsigned)(lane >> 3) * 1024u + 16u * ((lane & 7) ^ (((lane >> 4) & 1) << 2));
  const char* wdb = (const char*)A.wd + T.nt2 * 128 + l * 8192;
  auto wd_units = [&](int u0, int u1, unsigned dst) {
#pragma nounroll
    for (int u = u0; u < u1; ++u) { const int ur = u + T.rot2 >= 86 ? u + T.rot2 - 86 : u + T.rot2; glds(wdv, wdb + (size_t)ur * 16384, dst + 2048u * (u - u0) + 1024u * l); wait_vm<LTHR>(); }
  };
  if (mode & PH_G1) {
    unsigned xv[2], wv[3]; u64 wm[3];
#pragma unroll
    for (int i = 0; i < 2; ++i) { const int row = i * 8 + (lane >> 3); xv[i] = (unsigned)row * 1024u + 16u * ((lane & 7) ^ ((row >> 1) & 7)); }
#pragma unroll
    for (int i = 0; i < 3; ++i) {
      const int cell = 4 * i + g4, a = cell / 3, pair = cell - 3 * a;
      wv[i] = (unsigned)(4 * a + q4) * 2752u + (unsigned)pair * 64u + 16u * (pp ^ (2 * l));
      wm[i] = __ballot(pair < T.np);
    }
    const char* xb = (const char*)A.x + (size_t)(T.mt * 128 + l * 64) * 1024;
    const char* wb = (const char*)(l ? A.wu : A.wg) + T.start * 64;
    unsigned slot = 0;
#pragma nounroll
    for (int s = 0; s < 8; ++s) {
      if (s >= 3 && ctl_get(C_FR0 + s % 3) < 8u * (s / 3)) {
        wait_vm<0>(); ctl_set(C_FULL0 + l, s);
        ctl_wait<1>(C_FR0 + s % 3, 8u * (s / 3));
      }
      const int sr = (s + T.rot1) & 7;
      STAMPL(32 + s);
#pragma unroll
      for (int g = 0; g < 4; ++g) {
#pragma unroll
        for (int i = 0; i < 2; ++i) { glds(xv[i], xb + g * 16384 + sr * 128, slot + (unsigned)(l * 8 + 2 * g + i) * 1024u); wait_vm<LTHR>(); }
#pragma unroll
        for (int i = 0; i < 3; ++i) {
          glds_m(wv[i], wb + (size_t)(sr * 64 + g * 16) * 2752, slot + 16384u + (unsigned)l * 12288u + (unsigned)(3 * g + i) * 1024u, wm[i]);
          wait_vm<LTHR>();
        }
        if (5 * (g + 1) >= LTHR && 5 * g < LTHR && s >= 1) { wait_vm<5 * ((LTHR + 4) / 5)>(); ctl_set(C_FULL0 + l, s); }
      }
      slot = slot == 2 * L_SLOT ? 0 : slot + L_SLOT;
      STAMPL(40 + s);
    }
    if (mode & PH_G2) {
      wd_units(0, 8, L_SPARE);
      if (ctl_get(C_FR0 + 2) < 16u) { wait_vm<8>(); ctl_set(C_FULL0 + l, 8); ctl_wait<1>(C_FR0 + 2, 16u); }
      wd_units(8, 20, 81920u);
      wait_vm<20>(); ctl_set(C_FULL0 + l, 8);
      wd_units(20, 28, 81920u + 12 * 2048u);
      ctl_wait<1>(C_FR0, 24u);
      wd_units(28, 48, 0u);
      wait_vm<20>(); ctl_set(C_FULL0 + l, 10); STAMPL(48);
      ctl_wait<1>(C_FR0 + 1, 24u);
      STAMPL(49);
      wd_units(48, 68, 40960u);
      wait_vm<0>(); ctl_set(C_FULL0 + l, 12); STAMPL(50);
    } else {
      wait_vm<0>(); ctl_set(C_FULL0 + l, 8);
    }
  } else {
    if (mode & PH_G2) {
      wd_units(0, 8, L_SPARE); wd_units(8, 28, 81920u); wd_units(28, 48, 0u); wd_units(48, 68, 40960u);
      wait_vm<0>(); ctl_set(C_FULL0 + l, 12);
    }
  }
  if (mode & PH_G2) {
    ctl_wait<1>(C_FREE2, 8u);
    STAMPL(51);
    wd_units(68, 76, L_SPARE); wd_units(76, 86, 81920u);
    wait_vm<0>(); ctl_set(C_FULL0 + l, 13); STAMPL(52);
  }
}

template <int mode> DI void help_role(const Args& A, int hI, int lane, rsrc_t ws, u32x2 epoch) {
  const int bid = blockIdx.x;
  constexpr bool WAIT = mode & PH_WAIT;
  const Tile T = tile_of(bid);
  u32x2 tag = {0u, 0u};
  if (WAIT) tag = make_tag(epoch);
  if (hI == 0) {
    if (WAIT) {
      ctl_wait<2>(C_FPUB, 4u);
      poll_flags(ws, WS_FA + 2048u * (bid & 7), 256, tag, lane);
      ctl_set(C_AREADY, 1u);
      ctl_set(C_PAUSE, 0u);
      if (bid == 0 && lane == 0) {
        const u64 e1 = (((u64)epoch.y << 32) | epoch.x) + 1ull;
        st8(ws, WS_EPOCH, u32x2{(unsigned)e1, (unsigned)(e1 >> 32)});
      }
    }
    if (mode & PH_G1) {
      const int n1 = T.mt * 128 + lane;
      const u32x2 s0 = ld8(ws, WS_STATS + 64u * (n1 & 255) + 8u * (n1 >> 8)), s1 = ld8(ws, WS_STATS + 64u * ((n1 + 64) & 255) + 8u * (n1 >> 8));
      *(LAS u32x2*)ldsp(L_STAT + 8u * lane) = s0; *(LAS u32x2*)ldsp(L_STAT + 512u + 8u * lane) = s1;
      ctl_set(C_MM, 1u);
    }
  } else if (WAIT) {
    ctl_wait<2>(C_EPUB, 8u);
    poll_flags(ws, WS_FE + 2048u * (bid & 7), 256, tag, lane);
    ctl_set(C_EREADY, 1u);
    ctl_wait<2>(C_FPUB, 8u);
    poll_flags(ws, WS_FC + 2048u * (bid & 7), 256, tag, lane);
    ctl_set(C_CREADY, 1u);
    ctl_set(C_PAUSE, 0u);
  }
  if (mode & PH_G2) {
    const int tok0 = T.mt2 * 64 + hI * 32 + (lane >> 3);
    u32x4 xq[4], sq[4];
#pragma unroll
    for (int i = 0; i < 4; ++i) xq[i] = *(const u32x4*)(A.x + (size_t)(tok0 + 8 * i) * D_ + T.nt2 * 64 + (lane & 7) * 8);
    if (WAIT) {
      if (hI == 0) { ctl_wait<2>(C_GPUB, 8u); poll_flags(ws, WS_FB + 128u * (T.mt2 >> 1), 16, tag, lane); ctl_set(C_HREADY, 1u); }
      ctl_wait<2>(C_FPUB, 12u);
      poll_flags(ws, WS_FD + 2048u * (bid & 7) + 8u * ((T.mt2 * 64 + hI * 32) & 255), 32, tag, lane);
    }
#pragma unroll
    for (int i = 0; i < 4; ++i) sq[i] = ld16(ws, WS_S + (unsigned)(tok0 + 8 * i) * 1024u + (unsigned)T.nt2 * 128u + 16u * (lane & 7));
    if (mode & PH_P3) ctl_wait<1>(C_FDONE, 4u);
    const unsigned lo = L_FFT + 4096u * hI + 16u * lane;
#pragma unroll
    for (int i = 0; i < 4; ++i) { *(LAS u32x4*)ldsp(lo + 1024u * i) = xq[i]; *(LAS u32x4*)ldsp(lo + 8192u + 1024u * i) = sq[i]; }
    ctl_add(C_SXREADY);
  }
}

DI void csum(bf16x8 f, float& lo, float& hi) {
  const u32x4 u = __builtin_bit_cast(u32x4, f);
  const unsigned a = u.x, b = u.y, c = u.z, d = u.w;
  float t0, t1, t2, t3, t4, t5, t6, t7;
  asm("v_lshlrev_b32 %0, 16, %10\n\tv_and_b32 %1, 0xffff0000, %10\n\tv_lshlrev_b32 %2, 16, %11\n\tv_and_b32 %3, 0xffff0000, %11\n\t"
      "v_lshlrev_b32 %4, 16, %12\n\tv_and_b32 %5, 0xffff0000, %12\n\tv_lshlrev_b32 %6, 16, %13\n\tv_and_b32 %7, 0xffff0000, %13\n\t"
      "v_add_f32 %0, %0, %2\n\tv_add_f32 %1, %1, %3\n\tv_add_f32 %4, %4, %6\n\tv_add_f32 %5, %5, %7\n\t"
      "v_add_f32 %8, %8, %0\n\tv_add_f32 %9, %9, %1\n\tv_add_f32 %8, %8, %4\n\tv_add_f32 %9, %9, %5"
      : "=&v"(t0), "=&v"(t1), "=&v"(t2), "=&v"(t3), "=&v"(t4), "=&v"(t5), "=&v"(t6), "=&v"(t7), "+v"(lo), "+v"(hi)
      : "v"(a), "v"(b), "v"(c), "v"(d));
}
template <int mode> DI void gemm_role(const Args& A, int t, rsrc_t ws, u32x2 epoch) {
  const int lane = t & 63, w = __builtin_amdgcn_readfirstlane(t >> 6);
  constexpr bool WAIT = mode & PH_WAIT;
  const Tile T = tile_of(blockIdx.x);
  const int r = lane & 31, h = lane >> 5;
  if (mode & PH_P0) {
    const int bid = blockIdx.x, rowc = bid & 7, kbp = (bid >> 3) & 15, side = bid >> 7;
    const int rr = t & 15, q = t >> 4, c = side ? (8 - rowc) & 7 : rowc;
    const int ro = (rowc + 8 * (2 * kbp + (rr >> 3) + 32 * (rr & 7))) * DF_;
    const int co = ro + (side ? 252 - 4 * q : 4 * q + 1);
    const U8 a = *(const U8*)(A.wr + co), b = *(const U8*)(A.wi + co);
    unsigned wd[4] = {__builtin_amdgcn_perm(b.a, a.a, 0x05040100u), __builtin_amdgcn_perm(b.a, a.a, 0x07060302u),
                      __builtin_amdgcn_perm(b.b, a.b, 0x05040100u), __builtin_amdgcn_perm(b.b, a.b, 0x07060302u)};
    if (q == 31 && (c >= 5 || ((c & 3) == 0 && side))) {
      const unsigned z0 = (unsigned)A.wr[ro] | ((unsigned)A.wi[ro] << 16), z1 = (unsigned)A.wr[ro + 256] | ((unsigned)A.wi[ro + 256] << 16);
      if (side) wd[0] = z0; else wd[3] = z0;
      st4(ws, WS_W2 + (unsigned)(2 * c + side) * 1024u + 64u * kbp + 4u * rr, z1);
    }
    const unsigned o = WS_W + (unsigned)(q * 8 + c) * 8192u + (unsigned)side * 4096u + 64u * kbp + 4u * rr;
#pragma unroll
    for (int z = 0; z < 4; ++z) st4(ws, o + 1024u * (side ? 3 - z : z), wd[z]);
    if (WAIT) publish<8>(ws, C_EPUB, 8, WS_FE + 8u * bid, make_tag(epoch), lane);
  }
  if (mode & PH_G1) {
    const int tg = w & 3, fg = w >> 2;
    const int xrow = tg * 32 + r;
    const unsigned xa = ((unsigned)xrow * 128u + 16u * (h ^ ((xrow >> 1) & 7))) ^ (32u * tg);
    const unsigned wa = wfrag_off(lane, fg);
    f32x16 acc[3];
#pragma unroll
    for (int p = 0; p < 3; ++p)
#pragma unroll
      for (int i = 0; i < 16; ++i) acc[p][i] = 0.f;
    float cl[3] = {0.f, 0.f, 0.f}, ch[3] = {0.f, 0.f, 0.f};
    unsigned slot = 0;
    STAMP(0);
#pragma nounroll
    for (int s = 0; s < 8; ++s) {
      full_wait<1>(s + 1);
      if (s == 0) STAMP(1);
      if (s == 1) STAMP(2);
      if (s == 4) STAMP(3);
      const unsigned xs = slot + xa;
      bf16x8 xf[4], wf[4][3];
      auto ldk = [&](int ks) {
        const unsigned wsl = wa + (slot + 3072u * (ks ^ tg));
        xf[ks] = *(LAS bf16x8*)ldsp(xs ^ (32u * ks));
#pragma unroll
        for (int p = 0; p < 3; ++p) wf[ks][p] = tr2(wsl + p * 256u, wsl + 768u + p * 256u);
      };
      ldk(0);
#pragma unroll
      for (int ks = 0; ks < 4; ++ks) {
        if (ks < 3) ldk(ks + 1); else ctl_add(C_FR0 + s % 3);
        __builtin_amdgcn_sched_barrier(0);
#pragma unroll
        for (int p = 0; p < 2; ++p) acc[p] = MFMA(wf[ks][p], xf[ks], acc[p]);
        if (T.np == 3) acc[2] = MFMA(wf[ks][2], xf[ks], acc[2]);
        if (ks < 3) csum(wf[0][ks], cl[ks], ch[ks]);
        if (ks == 2 && s == 7) {
#pragma unroll
          for (int p = 0; p < 3; ++p) {
            const float cs = sadd(cl[p], ch[p]);
            auto sw = __builtin_amdgcn_permlane32_swap(__float_as_uint(cs), __float_as_uint(cs), false, false);
            *(LAS float*)ldsp(L_CS + 1536u * fg + 384u * tg + 128u * p + 4u * r) = sadd(__uint_as_float(sw[0]), __uint_as_float(sw[1]));
          }
          ctl_add(C_CS0 + fg);
        }
        __builtin_amdgcn_sched_barrier(0);
      }
      slot = slot == 2 * L_SLOT ? 0 : slot + L_SLOT;
    }
    STAMP(4);
    ctl_wait<1>(C_MM, 1u);
    ctl_wait<0>(C_CS0 + fg, 4u);
    const cf st = lds_ld(L_STAT + 8u * xrow);
    const float m0 = rbf(-st.x), mr = -st.x - m0, m1 = rbf(mr), m2 = mr - m1;
    const u32x4 bq = {pk2(m0, m0), pk2(m0, m1), pk2(m1, m2), 0u};
    const bf16x8 bfrag = __builtin_bit_cast(bf16x8, bq);
    constexpr float K0 = -2.0f * 0.7978845608028654f * 1.4426950408889634f, K1 = K0 * 0.044715f;
    const float k0 = K0 * st.y, k1 = K1 * st.y * st.y * st.y, ir2 = __builtin_amdgcn_rcpf(st.y * st.y);
#pragma unroll
    for (int p = 0; p < 3; ++p) {
      if (p < T.np) {
        const unsigned ca = L_CS + 1536u * fg + 128u * p + 4u * r;
        const float co = (*(LAS float*)ldsp(ca) + *(LAS float*)ldsp(ca + 384u)) + (*(LAS float*)ldsp(ca + 768u) + *(LAS float*)ldsp(ca + 1152u));
        const float ct = h ? 0.f : co;
        const float c0 = rbf(ct), r1 = ct - c0, c1 = rbf(r1), c2 = r1 - c1;
        const u32x4 aq = {pk2(c0, c1), pk2(c2, c0), pk2(c1, c0), 0u};
        const f32x16 a = MFMA(__builtin_bit_cast(bf16x8, aq), bfrag, acc[p]);
        unsigned hv[4];
#pragma unroll
        for (int i = 0; i < 4; ++i) {
          const cf g = {a[2 * i], a[2 * i + 1]}, u = {a[8 + 2 * i], a[9 + 2 * i]};
          const cf z = g * (g * g * k1 + k0);
          const cf d = cf{__builtin_amdgcn_exp2f(z.x), __builtin_amdgcn_exp2f(z.y)} * ir2 + ir2;
          const cf hh = (g * u) * cf{__builtin_amdgcn_rcpf(d.x), __builtin_amdgcn_rcpf(d.y)};
          hv[i] = pk2(hh.x, hh.y);
        }
        const int unit = 2 * (T.start + p) + fg;
        st16(ws, WS_H + (unsigned)((T.mt * 4 + tg) * 86 + unit) * 1024u + 16u * lane, u32x4{hv[0], hv[1], hv[2], hv[3]});
      }
    }
    STAMP(5);
    if (WAIT) publish<1>(ws, C_GPUB, 8, WS_FB + 8u * (T.mt * 16 + T.nt), make_tag(epoch), lane);
  }
  if (mode & PH_G2) {
    const int th = w & 1, kq = w >> 1;
    const int g4 = lane >> 4, q4 = (lane >> 2) & 3, pp = lane & 3;
    const int dsub = g4 & 1, hh = g4 >> 1;
    unsigned wa2[2];
#pragma unroll
    for (int dh = 0; dh < 2; ++dh)
      wa2[dh] = (unsigned)kq * 2048u + (unsigned)(4 * hh + q4) * 128u + (unsigned)(dh ^ (q4 >> 1)) * 64u + (unsigned)dsub * 32u + 8u * pp;
    const unsigned wa3[2] = {wa2[0] + 81920u, wa2[1] + 81920u};
    f32x16 acc[2];
#pragma unroll
    for (int p = 0; p < 2; ++p)
#pragma unroll
      for (int i = 0; i < 16; ++i) acc[p][i] = 0.f;
    STAMP(7);
    if (WAIT) ctl_wait<1>(C_HREADY, 1u);
    STAMP(9);
    const unsigned ho = WS_H + (unsigned)((2 * T.mt2 + th) * 86) * 1024u + 16u * lane;
    auto hoff = [&](int i) { const int u = 4 * i + kq + T.rot2; return (unsigned)(u >= 86 ? u - 86 : u) * 1024u; };
    auto hld = [&](int i) { return __builtin_amdgcn_raw_buffer_load_b128(ws, ho, hoff(i == 21 && kq >= 2 ? 20 : i), 16); };
    constexpr int WIN = HWIN;
    u32x4 hf[22];
#pragma unroll
    for (int i = 0; i < WIN; ++i) hf[i] = hld(i);
    full_wait<1>(10);
#pragma unroll
    for (int i = 0; i < 22; ++i) {
      if (i == 1) STAMP(10);
      if (i == 17) STAMP(11);
      if (i == 4) full_wait<1>(10);
      if (i == 7 || i == 12) full_wait<1>(12);
      if (i == 17) full_wait<1>(13);
      if (i + WIN < 22) hf[i + WIN] = hld(i + WIN);
      if (i < 21 || kq < 2) {
        constexpr unsigned dummy = 0;
        const unsigned ad = wd_addr(4 * i) + dummy;
        const unsigned b0 = ad >= 81920u ? wa3[0] : wa2[0], b1 = ad >= 81920u ? wa3[1] : wa2[1], so = ad >= 81920u ? ad - 81920u : ad;
        bf16x8 wf0, wf1;
        if (so + 1024u < 65536u - 8192u) { wf0 = tr2(b0 + so, b0 + so + 1024u); wf1 = tr2(b1 + so, b1 + so + 1024u); }
        else { wf0 = tr2(b0 + so, b0 + so + 1024u); wf1 = tr2(b1 + so, b1 + so + 1024u); }
        if (i == 4) ctl_add(C_FREE2);
        const bf16x8 hb = __builtin_bit_cast(bf16x8, hf[i]);
        acc[0] = MFMA(wf0, hb, acc[0]);
        acc[1] = MFMA(wf1, hb, acc[1]);
      }
      fence_c();
    }
    STAMP(12);
    ctl_add(C_RED);
    ctl_wait<0>(C_RED, 8u);
    const unsigned pw = (unsigned)kq * 17408u + (unsigned)(th * 32 + r) * 272u + 16u * h;
#pragma unroll
    for (int dh = 0; dh < 2; ++dh)
#pragma unroll
      for (int g = 0; g < 4; ++g)
        *(LAS f32x4*)ldsp(pw + dh * 128u + g * 32u) = f32x4{acc[dh][4 * g], acc[dh][4 * g + 1], acc[dh][4 * g + 2], acc[dh][4 * g + 3]};
    ctl_add(C_RED);
    ctl_wait<0>(C_RED, 16u);
    const int tok = t >> 3, d8 = t & 7;
    const unsigned pr = (unsigned)tok * 272u + 32u * d8;
    f32x4 o0 = *(LAS f32x4*)ldsp(pr), o1 = *(LAS f32x4*)ldsp(pr + 16);
#pragma unroll
    for (int z = 1; z < 4; ++z) { o0 += *(LAS f32x4*)ldsp(pr + z * 17408u); o1 += *(LAS f32x4*)ldsp(pr + z * 17408u + 16); }
    const float ov[8] = {o0.x, o0.y, o0.z, o0.w, o1.x, o1.y, o1.z, o1.w};
    float gs[8];
#pragma unroll
    for (int i = 0; i < 8; ++i) gs[i] = ov[i] * __builtin_amdgcn_rcpf(1.0f + __builtin_amdgcn_exp2f(-1.4426950408889634f * ov[i]));
    STAMP(13);
    ctl_wait<1>(C_SXREADY, 2u);
    STAMP(14);
    const u32x4 xq = *(LAS u32x4*)ldsp(L_FFT + 16u * t), sq = *(LAS u32x4*)ldsp(L_FFT + 8192u + 16u * t);
    u32x4 res;
#pragma unroll
    for (int i = 0; i < 4; ++i) {
      const unsigned sx = sq[i], xx = xq[i];
      res[i] = pk2(bflo(sx) * gs[2 * i] + bflo(xx), bfhi(sx) * gs[2 * i + 1] + bfhi(xx));
    }
    *(u32x4*)(A.out + (size_t)(T.mt2 * 64 + tok) * D_ + T.nt2 * 64 + d8 * 8) = res;
    STAMP(15);
  }
}

template <int MODE>
__global__ __launch_bounds__(1024, 4) void fused_kernel(Args A) {
  const int tid = threadIdx.x, wv = __builtin_amdgcn_readfirstlane(tid >> 6);
  if (wv == 15) { *(volatile LAS unsigned*)ldsp(L_CTL + 4u * (tid & 63)) = ((tid & 63) == C_PAUSE && !(MODE & PH_WAIT)) ? ~0u : 0u; wait_lgkm(); }
  __builtin_amdgcn_s_barrier();
  if (wv == 4 || wv == 5) {
    __builtin_amdgcn_s_setprio(2);
    STAMP_INIT(32, 21);
    load_role<MODE>(A, wv - 4, tid & 63);
    STAMP_DUMP(32, 21);
    return;
  }
  const rsrc_t ws = __builtin_amdgcn_make_buffer_rsrc(A.ws, 0, WS_END, 0x00020000);
  u32x2 epoch = {0u, 0u};
  if (MODE & PH_WAIT) epoch = ld8(ws, WS_EPOCH);
  if (wv >= 8) {
    if (wv == 8) STAMP_INIT(0, 16);
    gemm_role<MODE>(A, tid - 512, ws, epoch);
    if (wv == 8) STAMP_DUMP(0, 16);
  } else if (wv < 4) {
    __builtin_amdgcn_s_setprio(3);
    if (wv == 0) STAMP_INIT(16, 16);
    fft_role<MODE>(A, tid, ws, epoch);
    if (wv == 0) STAMP_DUMP(16, 16);
  } else {
    help_role<MODE>(A, wv - 6, tid & 63, ws, epoch);
  }
}

extern "C" void kernel_launch(void* const* d_in, const int* in_sizes, int n_in, void* d_out, int out_size, void* d_ws,
                              size_t ws_size, hipStream_t stream) {
  Args A;
  A.x = (const uint16_t*)d_in[0]; A.wr = (const uint16_t*)d_in[1]; A.wi = (const uint16_t*)d_in[2];
  A.wg = (const uint16_t*)d_in[4]; A.wu = (const uint16_t*)d_in[5]; A.wd = (const uint16_t*)d_in[6];
  A.out = (uint16_t*)d_out; A.ws = (char*)d_ws;
#define LAUNCH(M) do { (void)hipFuncSetAttribute((const void*)fused_kernel<M>, hipFuncAttributeMaxDynamicSharedMemorySize, L_TOTAL); \
    fused_kernel<M><<<256, 1024, L_TOTAL, stream>>>(A); } while (0)
  LAUNCH(PH_P0 | PH_G1 | PH_P2 | PH_P3 | PH_G2 | PH_WAIT);
}
```

```cpp
#include <hip/hip_runtime.h>
#include <stdint.h>

#define MULTI_LAUNCH 0
#define LTHR 20
#define PSLEEP 1
#define ROT1 0
#define GSLEEP 2
#define LSPIN 4
#define LDELAY 8
#define LDELAY2 16
#define PRETOUCH 0
#define CODE_LINES 224
#define HWIN 8

#define DI __device__ __forceinline__
#define LAS __attribute__((address_space(3)))
typedef __attribute__((ext_vector_type(2))) float cf;
typedef __attribute__((ext_vector_type(4))) float f32x4;
typedef __attribute__((ext_vector_type(16))) float f32x16;
typedef __attribute__((ext_vector_type(8))) short bf16x8;
typedef __attribute__((ext_vector_type(4))) short s16x4;
typedef __attribute__((ext_vector_type(2))) __bf16 bf16x2;
typedef __attribute__((ext_vector_type(4))) unsigned u32x4;
typedef __attribute__((ext_vector_type(2))) unsigned u32x2;
typedef unsigned long long u64;

constexpr int D_ = 512, DF_ = 257;
constexpr unsigned WS_EPOCH = 0;
constexpr unsigned WS_FB = 4096;
constexpr unsigned WS_FA = 131072, WS_FC = 147456, WS_FD = 163840, WS_FE = 180224;
constexpr unsigned WS_GO = 8192;
constexpr unsigned WS_CS = 262144;
constexpr unsigned WS_STATS = 32768;
constexpr unsigned WS_W2 = 65536;
constexpr unsigned WS_X = 1u << 20;
constexpr unsigned WS_Z = 5u << 20;
constexpr unsigned WS_W = 9u << 20;
constexpr unsigned WS_S = 11u << 20;
constexpr unsigned WS_H = 13u << 20;
constexpr unsigned WS_END = 20u << 20;
constexpr unsigned L_SLOT = 40960;
constexpr unsigned L_SPARE = 122880;
constexpr unsigned L_CS = 139264;
constexpr unsigned L_FFT = 143360;
constexpr unsigned L_STAT = L_FFT + 18688;
constexpr unsigned L_ST8 = L_STAT + 1024;
constexpr unsigned L_CTL = L_ST8 + 64;
constexpr unsigned L_TOTAL = L_CTL + 704;
static_assert(L_TOTAL <= 163840, "LDS");
constexpr int EP = 292, KP = 36;
enum { C_FULL0 = 0  , C_PAUSE = 4, C_FSYNC = 5, C_FPUB = 6, C_GPUB = 7, C_AREADY = 8, C_CREADY = 9, C_HREADY = 10,
       C_SXREADY = 11, C_RED = 12, C_EPUB = 13, C_FREE2 = 14, C_MM = 15, C_EREADY = 16, C_FDONE = 17, C_FR0 = 18  ,
       C_FEND = 21, C_CSW = 22, C_FREE2A = 23, C_CSR = 24 };
enum { PH_P0 = 1, PH_G1 = 2, PH_P2 = 4, PH_P3 = 8, PH_G2 = 16, PH_WAIT = 32 };
constexpr unsigned SPIN_CAP = 1u << 14;

struct Args {
  const uint16_t *x, *wr, *wi, *wg, *wu, *wd;
  uint16_t* out;
  char* ws;
};

DI float bflo(unsigned u) { return __uint_as_float(u << 16); }
DI float bfhi(unsigned u) { return __uint_as_float(u & 0xffff0000u); }
DI unsigned pk2(float a, float b) { cf v = {a, b}; bf16x2 c = __builtin_convertvector(v, bf16x2); return __builtin_bit_cast(unsigned, c); }
DI float rbf(float a) { return bflo(pk2(a, a)); }
DI cf cis(float rev) { return cf{__builtin_amdgcn_cosf(rev), __builtin_amdgcn_sinf(rev)}; }

#define SUBM "neg_lo:[0,1] neg_hi:[0,1]"
#define SWP "op_sel:[0,1] op_sel_hi:[1,0]"
#define ADDROT_F SWP " neg_hi:[0,1]"
#define SUBROT_F SWP " neg_lo:[0,1]"
#define ADDROT_I SWP " neg_lo:[0,1]"
#define SUBROT_I SWP " neg_hi:[0,1]"
#define DFT8_BODY(AR, SR, RSUB, FAR, FSR)                                                                          \
  "v_pk_add_f32 %8, %0, %4\n\tv_pk_add_f32 %12, %0, %4 " SUBM "\n\t"                                               \
  "v_pk_add_f32 %9, %1, %5\n\tv_pk_add_f32 %13, %1, %5 " SUBM "\n\t"                                               \
  "v_pk_add_f32 %10, %2, %6\n\tv_pk_add_f32 %14, %2, %6 " SUBM "\n\t"                                              \
  "v_pk_add_f32 %11, %3, %7\n\tv_pk_add_f32 %15, %3, %7 " SUBM "\n\t"                                              \
  "v_pk_add_f32 %1, %13, %13 " AR "\n\t"                                                                           \
  "v_pk_add_f32 %0, %8, %10\n\tv_pk_add_f32 %2, %8, %10 " SUBM "\n\t"                                              \
  "v_pk_add_f32 %3, %15, %15 " RSUB "\n\t"                                                                         \
  "v_pk_add_f32 %4, %9, %11\n\tv_pk_add_f32 %6, %9, %11 " SUBM "\n\t"                                              \
  "v_pk_add_f32 %5, %12, %14 " AR "\n\tv_pk_add_f32 %7, %12, %14 " SR "\n\t"                                       \
  "v_pk_add_f32 %8, %1, %3\n\tv_pk_add_f32 %9, %1, %3 " SUBM "\n\t"                                                \
  "v_pk_add_f32 %12, %0, %4\n\tv_pk_add_f32 %13, %0, %4 " SUBM "\n\t"                                              \
  "v_pk_add_f32 %14, %2, %6 " AR "\n\tv_pk_add_f32 %15, %2, %6 " SR "\n\t"                                         \
  "v_pk_fma_f32 %10, %8, %16, %5\n\tv_pk_fma_f32 %11, %9, %16, %7 " FAR "\n\t"                                     \
  "v_pk_fma_f32 %8, %8, %16, %5 neg_lo:[1,0,0] neg_hi:[1,0,0]\n\tv_pk_fma_f32 %9, %9, %16, %7 " FSR
#define FSWP "op_sel:[1,0,0] op_sel_hi:[0,1,1]"
template <int INV> DI void dft8(cf (&v)[8]) {
  const u64 R = 0x3F3504F33F3504F3ull;
  cf t0, t1, t2, t3, t4, t5, t6, t7;
  if (!INV)
    asm(DFT8_BODY(ADDROT_F, SUBROT_F, SWP " neg_lo:[1,0] neg_hi:[1,1]", FSWP " neg_hi:[1,0,0]", FSWP " neg_lo:[1,0,0]")
        : "+v"(v[0]), "+v"(v[1]), "+v"(v[2]), "+v"(v[3]), "+v"(v[4]), "+v"(v[5]), "+v"(v[6]), "+v"(v[7]),
          "=&v"(t0), "=&v"(t1), "=&v"(t2), "=&v"(t3), "=&v"(t4), "=&v"(t5), "=&v"(t6), "=&v"(t7) : "s"(R));
  else
    asm(DFT8_BODY(ADDROT_I, SUBROT_I, SWP " neg_lo:[1,1] neg_hi:[1,0]", FSWP " neg_lo:[1,0,0]", FSWP " neg_hi:[1,0,0]")
        : "+v"(v[0]), "+v"(v[1]), "+v"(v[2]), "+v"(v[3]), "+v"(v[4]), "+v"(v[5]), "+v"(v[6]), "+v"(v[7]),
          "=&v"(t0), "=&v"(t1), "=&v"(t2), "=&v"(t3), "=&v"(t4), "=&v"(t5), "=&v"(t6), "=&v"(t7) : "s"(R));
  v[0] = t4; v[1] = t2; v[2] = t6; v[3] = t3; v[4] = t5; v[5] = t0; v[6] = t7; v[7] = t1;
}
#define DFT4X2_BODY(AR, SR)                                                                                        \
  "v_pk_add_f32 %8, %0, %2\n\tv_pk_add_f32 %9, %0, %2 " SUBM "\n\t"                                                \
  "v_pk_add_f32 %10, %1, %3\n\tv_pk_add_f32 %11, %1, %3 " SUBM "\n\t"                                              \
  "v_pk_add_f32 %12, %4, %6\n\tv_pk_add_f32 %13, %4, %6 " SUBM "\n\t"                                              \
  "v_pk_add_f32 %14, %5, %7\n\tv_pk_add_f32 %15, %5, %7 " SUBM "\n\t"                                              \
  "v_pk_add_f32 %0, %8, %10\n\tv_pk_add_f32 %2, %8, %10 " SUBM "\n\t"                                              \
  "v_pk_add_f32 %1, %9, %11 " AR "\n\tv_pk_add_f32 %3, %9, %11 " SR "\n\t"                                         \
  "v_pk_add_f32 %4, %12, %14\n\tv_pk_add_f32 %6, %12, %14 " SUBM "\n\t"                                            \
  "v_pk_add_f32 %5, %13, %15 " AR "\n\tv_pk_add_f32 %7, %13, %15 " SR
template <int INV> DI void dft4x2(cf (&v)[8]) {
  cf t0, t1, t2, t3, t4, t5, t6, t7;
  if (!INV)
    asm(DFT4X2_BODY(ADDROT_F, SUBROT_F)
        : "+v"(v[0]), "+v"(v[1]), "+v"(v[2]), "+v"(v[3]), "+v"(v[4]), "+v"(v[5]), "+v"(v[6]), "+v"(v[7]),
          "=&v"(t0), "=&v"(t1), "=&v"(t2), "=&v"(t3), "=&v"(t4), "=&v"(t5), "=&v"(t6), "=&v"(t7));
  else
    asm(DFT4X2_BODY(ADDROT_I, SUBROT_I)
        : "+v"(v[0]), "+v"(v[1]), "+v"(v[2]), "+v"(v[3]), "+v"(v[4]), "+v"(v[5]), "+v"(v[6]), "+v"(v[7]),
          "=&v"(t0), "=&v"(t1), "=&v"(t2), "=&v"(t3), "=&v"(t4), "=&v"(t5), "=&v"(t6), "=&v"(t7));
}
#define TWM(t, a, w) "v_pk_mul_f32 %" #t ", %" #a ", %" #w " op_sel_hi:[1,0]\n\t"
#define TWF(t, a, w, N) "v_pk_fma_f32 %" #t ", %" #a ", %" #w ", %" #t " op_sel:[1,1,0] op_sel_hi:[0,1,1] " N "\n\t"
#define TW7_BODY(N)                                                                                                \
  "s_nop 0\n\t" TWM(0, 7, 14) TWM(1, 8, 15) TWM(2, 9, 16) TWM(3, 10, 17) TWM(4, 11, 18) TWM(5, 12, 19) TWM(6, 13, 20) \
  TWF(0, 7, 14, N) TWF(1, 8, 15, N) TWF(2, 9, 16, N) TWF(3, 10, 17, N) TWF(4, 11, 18, N) TWF(5, 12, 19, N) TWF(6, 13, 20, N)
template <int INV> DI void tw7(cf (&v)[8], const cf (&w)[7]) {
  cf t0, t1, t2, t3, t4, t5, t6;
  if (!INV)
    asm(TW7_BODY("neg_lo:[0,1,0]")
        : "=&v"(t0), "=&v"(t1), "=&v"(t2), "=&v"(t3), "=&v"(t4), "=&v"(t5), "=&v"(t6)
        : "v"(v[1]), "v"(v[2]), "v"(v[3]), "v"(v[4]), "v"(v[5]), "v"(v[6]), "v"(v[7]),
          "v"(w[0]), "v"(w[1]), "v"(w[2]), "v"(w[3]), "v"(w[4]), "v"(w[5]), "v"(w[6]));
  else
    asm(TW7_BODY("neg_hi:[0,1,0]")
        : "=&v"(t0), "=&v"(t1), "=&v"(t2), "=&v"(t3), "=&v"(t4), "=&v"(t5), "=&v"(t6)
        : "v"(v[1]), "v"(v[2]), "v"(v[3]), "v"(v[4]), "v"(v[5]), "v"(v[6]), "v"(v[7]),
          "v"(w[0]), "v"(w[1]), "v"(w[2]), "v"(w[3]), "v"(w[4]), "v"(w[5]), "v"(w[6]));
  v[1] = t0; v[2] = t1; v[3] = t2; v[4] = t3; v[5] = t4; v[6] = t5; v[7] = t6;
}
#define FL1(r, p, a) "v_pk_mul_f32 %" #r ", %" #p ", %" #a " op_sel_hi:[1,0]\n\t"
#define FL2(r, p, a) "v_pk_fma_f32 %" #r ", %" #p ", %" #a ", %" #r " op_sel:[1,1,0] op_sel_hi:[0,1,1] neg_lo:[0,1,0]\n\t"
#define FL3(r, b, q) "v_pk_fma_f32 %" #r ", %" #b ", %" #q ", %" #r " op_sel_hi:[1,0,1]\n\t"
#define FL4(r, b, q) "v_pk_fma_f32 %" #r ", %" #b ", %" #q ", %" #r " op_sel:[1,1,0] op_sel_hi:[0,1,1] neg_hi:[0,1,0]\n\t"
DI void filt4(cf* v, const cf* q, const cf* al, const cf* be) {
  cf r0, r1, r2, r3;
  asm(FL1(0, 4, 12) FL1(1, 5, 13) FL1(2, 6, 14) FL1(3, 7, 15) FL2(0, 4, 12) FL2(1, 5, 13) FL2(2, 6, 14) FL2(3, 7, 15)
      FL3(0, 16, 8) FL3(1, 17, 9) FL3(2, 18, 10) FL3(3, 19, 11) FL4(0, 16, 8) FL4(1, 17, 9) FL4(2, 18, 10) FL4(3, 19, 11)
      : "=&v"(r0), "=&v"(r1), "=&v"(r2), "=&v"(r3)
      : "v"(v[0]), "v"(v[1]), "v"(v[2]), "v"(v[3]), "v"(q[0]), "v"(q[1]), "v"(q[2]), "v"(q[3]),
        "v"(al[0]), "v"(al[1]), "v"(al[2]), "v"(al[3]), "v"(be[0]), "v"(be[1]), "v"(be[2]), "v"(be[3]));
  v[0] = r0; v[1] = r1; v[2] = r2; v[3] = r3;
}
#define CNDD(d, s0, s1, QP) "v_cndmask_b32_dpp %" #d ", %" #s0 ", %" #s1 ", vcc quad_perm:" QP " row_mask:0xf bank_mask:0xf\n\t"
#define SW4_BODY(QP)                                                                                               \
  "s_mov_b64 vcc, %16\n\ts_nop 0\n\t" CNDD(4, 8, 12, QP) CNDD(5, 9, 13, QP) CNDD(6, 10, 14, QP) CNDD(7, 11, 15, QP)   \
  "s_not_b64 vcc, vcc\n\t" CNDD(0, 12, 8, QP) CNDD(1, 13, 9, QP) CNDD(2, 14, 10, QP) CNDD(3, 15, 11, QP)
template <int X> DI void sw4(float& a0, float& a1, float& a2, float& a3, float& b0, float& b1, float& b2, float& b3) {
  float c0, c1, c2, c3, d0, d1, d2, d3;
  if (X == 1) {
    const u64 m = 0xAAAAAAAAAAAAAAAAull;
    asm(SW4_BODY("[1,0,3,2]") : "=&v"(c0), "=&v"(c1), "=&v"(c2), "=&v"(c3), "=&v"(d0), "=&v"(d1), "=&v"(d2), "=&v"(d3)
        : "v"(a0), "v"(a1), "v"(a2), "v"(a3), "v"(b0), "v"(b1), "v"(b2), "v"(b3), "s"(m) : "vcc", "scc");
  } else {
    const u64 m = 0xCCCCCCCCCCCCCCCCull;
    asm(SW4_BODY("[2,3,0,1]") : "=&v"(c0), "=&v"(c1), "=&v"(c2), "=&v"(c3), "=&v"(d0), "=&v"(d1), "=&v"(d2), "=&v"(d3)
        : "v"(a0), "v"(a1), "v"(a2), "v"(a3), "v"(b0), "v"(b1), "v"(b2), "v"(b3), "s"(m) : "vcc", "scc");
  }
  a0 = c0; a1 = c1; a2 = c2; a3 = c3; b0 = d0; b1 = d1; b2 = d2; b3 = d3;
}
template <int X> DI void swc(cf& a0, cf& a1, cf& b0, cf& b1) {
  float p0 = a0.x, p1 = a0.y, p2 = a1.x, p3 = a1.y, q0 = b0.x, q1 = b0.y, q2 = b1.x, q3 = b1.y;
  sw4<X>(p0, p1, p2, p3, q0, q1, q2, q3);
  a0 = cf{p0, p1}; a1 = cf{p2, p3}; b0 = cf{q0, q1}; b1 = cf{q2, q3};
}
DI void quadT2(cf (&v)[8]) {
  swc<1>(v[0], v[2], v[1], v[3]); swc<1>(v[4], v[6], v[5], v[7]);
  swc<2>(v[0], v[1], v[2], v[3]); swc<2>(v[4], v[5], v[6], v[7]);
}
template <int CTRL> DI float dppf(float v) { return __int_as_float(__builtin_amdgcn_update_dpp(0, __float_as_int(v), CTRL, 0xf, 0xf, true)); }
template <int CTRL> DI unsigned dppu(unsigned v) { return (unsigned)__builtin_amdgcn_update_dpp(0, (int)v, CTRL, 0xf, 0xf, true); }
DI float red32(float v) {
  v += dppf<0xB1>(v); v += dppf<0x4E>(v); v += dppf<0x141>(v); v += dppf<0x140>(v);
  auto r = __builtin_amdgcn_permlane16_swap(__float_as_uint(v), __float_as_uint(v), false, false);
  return __uint_as_float(r[0]) + __uint_as_float(r[1]);
}
DI float sadd(float a, float b) { float r; asm("v_add_f32 %0, %1, %2" : "=v"(r) : "v"(a), "v"(b)); return r; }
DI int opaque(int v) { asm volatile("" : "+v"(v)); return v; }

#define STAMP(i)
#define STAMPL(i)
#define STAMPH(i)
#define STAMPC(i)
#define STAMP_INIT(lo, n)
#define STAMP_DUMP(lo, n)
DI LAS char* ldsp(unsigned off) { return (LAS char*)(uintptr_t)off; }
DI cf lds_ld(unsigned off) { return *(LAS cf*)ldsp(off); }
DI void lds_st(unsigned off, cf v) { *(LAS cf*)ldsp(off) = v; }
DI void wait_lgkm() { asm volatile("s_waitcnt lgkmcnt(0)" ::: "memory"); }
DI void fence_c() { asm volatile("" ::: "memory"); }
template <int N> DI void wait_vm() { asm volatile("s_waitcnt vmcnt(%0)" ::"i"(N) : "memory"); }
DI void ctl_add(int idx) {
  const unsigned a = L_CTL + 4 * idx, one = 1u;
  asm volatile("s_waitcnt lgkmcnt(0)\n\ts_mov_b64 exec, 1\n\tds_add_u32 %0, %1\n\ts_mov_b64 exec, -1" ::"v"(a), "v"(one) : "memory");
}
DI void ctl_add_nw(int idx) {
  const unsigned a = L_CTL + 4 * idx, one = 1u;
  asm volatile("s_mov_b64 exec, 1\n\tds_add_u32 %0, %1\n\ts_mov_b64 exec, -1" ::"v"(a), "v"(one) : "memory");
}
DI unsigned ctl_add_rtn(int idx) {
  const unsigned a = L_CTL + 4 * idx, one = 1u;
  unsigned r;
  asm volatile("s_waitcnt lgkmcnt(0)\n\ts_mov_b64 exec, 1\n\tds_add_rtn_u32 %0, %1, %2\n\ts_mov_b64 exec, -1\n\ts_waitcnt lgkmcnt(0)"
               : "=&v"(r) : "v"(a), "v"(one) : "memory");
  return __builtin_amdgcn_readfirstlane(r);
}
DI void ctl_set(int idx, unsigned v) {
  const unsigned a = L_CTL + 4 * idx;
  asm volatile("s_waitcnt lgkmcnt(0)\n\ts_mov_b64 exec, 1\n\tds_write_b32 %0, %1\n\ts_mov_b64 exec, -1" ::"v"(a), "v"(v) : "memory");
}
DI unsigned ctl_get(int idx) { return __builtin_amdgcn_readfirstlane(*(volatile LAS unsigned*)ldsp(L_CTL + 4 * idx)); }
template <int SLEEP> DI void ctl_wait(int idx, unsigned target) {
  const unsigned a = L_CTL + 4 * idx;
  unsigned v, sv, cnt;
  asm volatile("s_mov_b32 %2, 0x20000\n"
               "1:\n\tds_read_b32 %0, %3\n\ts_waitcnt lgkmcnt(0)\n\tv_readfirstlane_b32 %1, %0\n\ts_cmp_ge_u32 %1, %4\n\ts_cbranch_scc1 2f\n\t"
               "s_sleep %5\n\ts_sub_u32 %2, %2, 1\n\ts_cbranch_scc0 1b\n2:"
               : "=&v"(v), "=&s"(sv), "=&s"(cnt) : "v"(a), "s"(__builtin_amdgcn_readfirstlane(target)), "i"(SLEEP) : "memory", "scc");
}
template <int SLEEP> DI void full_wait(unsigned target, int pair) {
  const unsigned a = L_CTL + 8u * pair, b = L_CTL + 4u * C_PAUSE;
  unsigned v0, v1, v2, sv, cnt;
  asm volatile("s_mov_b32 %4, 0x20000\n"
               "1:\n\tds_read_b32 %0, %5\n\tds_read_b32 %1, %5 offset:4\n\tds_read_b32 %2, %8\n\ts_waitcnt lgkmcnt(0)\n\t"
               "v_min3_u32 %0, %0, %1, %2\n\ts_nop 0\n\tv_readfirstlane_b32 %3, %0\n\ts_cmp_ge_u32 %3, %6\n\ts_cbranch_scc1 2f\n\t"
               "s_sleep %7\n\ts_sub_u32 %4, %4, 1\n\ts_cbranch_scc0 1b\n2:"
               : "=&v"(v0), "=&v"(v1), "=&v"(v2), "=&s"(sv), "=&s"(cnt)
               : "v"(a), "s"(__builtin_amdgcn_readfirstlane(target)), "i"(SLEEP), "v"(b) : "memory", "scc");
}
DI void fft_end(unsigned target) { if (ctl_add_rtn(C_FEND) + 1 == target) ctl_set(C_PAUSE, ~0u); }
DI void glds(unsigned voff, const void* sbase, unsigned ldsdst) {
  unsigned keep;
  asm volatile("s_mov_b32 %0, m0\n\ts_mov_b32 m0, %3\n\ts_nop 0\n\tglobal_load_lds_dwordx4 %1, %2\n\ts_mov_b32 m0, %0"
               : "=&s"(keep) : "v"(voff), "s"(sbase), "s"(ldsdst) : "memory");
}
DI void glds_m(unsigned voff, const void* sbase, unsigned ldsdst, u64 mask) {
  unsigned keep;
  asm volatile("s_mov_b32 %0, m0\n\ts_mov_b32 m0, %3\n\ts_mov_b64 exec, %4\n\tglobal_load_lds_dwordx4 %1, %2\n\ts_mov_b64 exec, -1\n\ts_mov_b32 m0, %0"
               : "=&s"(keep) : "v"(voff), "s"(sbase), "s"(ldsdst), "s"(mask) : "memory");
}
typedef __amdgpu_buffer_rsrc_t rsrc_t;
DI u32x4 ld16(rsrc_t r, unsigned off) { return __builtin_amdgcn_raw_buffer_load_b128(r, off, 0, 16); }
DI u32x2 ld8(rsrc_t r, unsigned off) { return __builtin_amdgcn_raw_buffer_load_b64(r, off, 0, 16); }
DI void st16(rsrc_t r, unsigned off, u32x4 v) { __builtin_amdgcn_raw_buffer_store_b128(v, r, off, 0, 16); }
DI void st8(rsrc_t r, unsigned off, u32x2 v) { __builtin_amdgcn_raw_buffer_store_b64(v, r, off, 0, 16); }
DI void st4(rsrc_t r, unsigned off, unsigned v) { __builtin_amdgcn_raw_buffer_store_b32(v, r, off, 0, 16); }
DI u32x4 cc2(cf a, cf b) { return u32x4{__float_as_uint(a.x), __float_as_uint(a.y), __float_as_uint(b.x), __float_as_uint(b.y)}; }
DI cf u2c(unsigned a, unsigned b) { return cf{__uint_as_float(a), __uint_as_float(b)}; }

DI u32x2 make_tag(u32x2 epoch) {
  u64 z = (((u64)__builtin_amdgcn_readfirstlane(epoch.y) << 32) | (unsigned)__builtin_amdgcn_readfirstlane(epoch.x)) + 0x9E3779B97F4A7C15ull;
  z = (z ^ (z >> 30)) * 0xBF58476D1CE4E5B9ull; z = (z ^ (z >> 27)) * 0x94D049BB133111EBull; z ^= z >> 31;
  return u32x2{(unsigned)z, (unsigned)(z >> 32)};
}
template <int NREP> DI void publish(rsrc_t ws, int cidx, unsigned target, unsigned flag_off, u32x2 tag, int lane) {
  wait_vm<0>();
  const unsigned r = ctl_add_rtn(cidx);
  if (r + 1 == target && lane < NREP) st8(ws, flag_off + 2048u * lane, tag);
}
DI void poll_flags(rsrc_t ws, unsigned flag_off, int nflags, u32x2 tag, int lane) {
  const unsigned o = flag_off + 32u * lane;
  bool done = 4 * lane >= nflags;
  for (unsigned spins = 0;; ++spins) {
    fence_c();
    if (!done) {
      const u32x4 a = ld16(ws, o), b = ld16(ws, o + 16);
      done = a.x == tag.x && a.y == tag.y && a.z == tag.x && a.w == tag.y && b.x == tag.x && b.y == tag.y && b.z == tag.x && b.w == tag.y;
    }
    if (__all(done)) break;
    if (spins > SPIN_CAP) break;
    __builtin_amdgcn_s_sleep(PSLEEP);
  }
  fence_c();
}
DI void wait_all(rsrc_t ws, unsigned flags, int phase, u32x2 tag, int lane) {
  const int bid = blockIdx.x;
  const unsigned go = WS_GO + 1024u * phase + 128u * (bid & 7);
  if (bid < 8) {
    poll_flags(ws, flags + 2048u * bid, 256, tag, lane);
    if (lane == 0) st8(ws, go, tag);
  } else {
    for (unsigned spins = 0; spins < 4 * SPIN_CAP; ++spins) {
      fence_c();
      const u32x2 a = ld8(ws, go);
      if (a.x == tag.x && a.y == tag.y) break;
      __builtin_amdgcn_s_sleep(GSLEEP);
    }
    fence_c();
  }
}
DI void twiddles(cf (&w)[7], int pos, float scale) {
#pragma unroll
  for (int i = 1; i < 8; ++i) { w[i - 1] = cis(-(float)(pos * i) * scale); asm volatile("" : "+v"(w[i - 1])); }
}

struct Tile { int mt, nt, np, start, mt2, nt2, rot1, rot2; };
DI Tile tile_of(int bid) {
  Tile T;
  T.mt = (bid & 1) * 8 + ((bid >> 3) & 7); T.nt = ((bid >> 1) & 3) * 4 + (bid >> 6);
  T.np = ((T.nt & 3) == 3 || T.nt == 14) ? 2 : 3;
  T.start = 3 * T.nt - (T.nt >> 2) - (T.nt == 15 ? 1 : 0);
  T.mt2 = (bid & 7) * 4 + ((bid >> 3) & 3); T.nt2 = bid >> 5;
  T.rot1 = ROT1 ? (T.mt + 2 * T.nt) & 7 : 0;
  T.rot2 = (11 * T.nt2 + 22 * (T.mt2 & 3)) % 86;
  return T;
}
struct __attribute__((packed, aligned(2))) U8 { unsigned a, b; };
DI float smul(float a, float b) { float r; asm("v_mul_f32 %0, %1, %2" : "=v"(r) : "v"(a), "v"(b)); return r; }
DI float ssub(float a, float b) { float r; asm("v_sub_f32 %0, %1, %2" : "=v"(r) : "v"(a), "v"(b)); return r; }
DI float sfma(float a, float b, float c) { float r; asm("v_fma_f32 %0, %1, %2, %3" : "=v"(r) : "v"(a), "v"(b), "v"(c)); return r; }
DI void lane_swap(float& a, float& b, int lane) {
  const int sa = __builtin_amdgcn_readlane(__float_as_int(a), 59), sb = __builtin_amdgcn_readlane(__float_as_int(b), 63);
  a = lane == 59 ? __int_as_float(sb) : a;
  b = lane == 63 ? __int_as_float(sa) : b;
}
DI void special_swap(cf (&v)[8], int lane) {
#pragma unroll
  for (int r = 1; r < 4; ++r) {
    float ax = v[8 - r].x, ay = v[8 - r].y, bx = v[r].x, by = v[r].y;
    lane_swap(ax, bx, lane); lane_swap(ay, by, lane);
    v[8 - r] = cf{ax, ay}; v[r] = cf{bx, by};
  }
}
DI void twiddles_c(cf (&w)[7], int b, bool side) {
#pragma unroll
  for (int i = 1; i < 8; ++i) { w[i - 1] = cis(-(float)(b * (side ? 8 - i : i)) * (1.0f / 2048.0f)); asm volatile("" : "+v"(w[i - 1])); }
}

template <int mode> DI void fft_role(const Args& A, const int t_, rsrc_t ws, u32x2 epoch) {
  u32x2 tag = {0u, 0u};
  const int t = t_;
  const int lane = t & 63, w = __builtin_amdgcn_readfirstlane(t >> 6), bid = blockIdx.x;
  constexpr bool WAIT = mode & PH_WAIT;
  const int hf = lane >> 5;
  const int l5 = lane & 31, k1l = (lane >> 2) & 7, llo = lane & 3, kb = k1l + 8 * llo;
  unsigned fs = 0;
  auto fsync = [&]() { fs += 4; ctl_add(C_FSYNC); ctl_wait<0>(C_FSYNC, fs); };
  const int k0 = 4 * (t >> 3) + 1 + (t & 3);
  const int kt = (t & 4) ? (t == 255 ? 0 : 256 - k0) : k0;
  cf tw2[7];
  cf v[8];
  auto gather_cs = [&](int ln) {
    const Tile T = tile_of(bid);
    const unsigned co = WS_CS + 4u * ((w - 2) * 1408 + 32 * T.start + 2 * (ln < 48 ? ln : 0));
    u32x2 cv[8];
#pragma unroll
    for (int z = 0; z < 8; ++z) cv[z] = ld8(ws, co + 11264u * z);
    float a0 = 0.f, a1 = 0.f;
#pragma unroll
    for (int z = 0; z < 8; ++z) { a0 = sadd(a0, __uint_as_float(cv[z].x)); a1 = sadd(a1, __uint_as_float(cv[z].y)); }
    if (ln < 48) lds_st(L_CS + 384u * (w - 2) + 8u * ln, cf{a0, a1});
    ctl_add(C_CSR);
  };
  auto stat_ld = [&](int tt) { const int n1 = tile_of(bid).mt * 128 + (tt - 128); return ld8(ws, WS_STATS + 64u * (n1 & 255) + 8u * (n1 >> 8)); };
  if ((mode & PH_G1) && !(mode & PH_P2) && w >= 2) {
    gather_cs(lane);
    *(LAS u32x2*)ldsp(L_STAT + 8u * (t - 128)) = stat_ld(t); ctl_add(C_MM);
  }
  STAMP(16);
  if (mode & PH_P0) {
    const int ra = hf ? (w ? 8 - w : 4) : w, rn = (8 - ra) & 7;
    unsigned raw[8];
    const unsigned* xr = (const unsigned*)(A.x + (size_t)(bid + 256 * ra) * D_);
#pragma unroll
    for (int i = 0; i < 8; ++i) raw[i] = xr[l5 + 32 * i];
    cf twA[7], twc[7];
    twiddles(twA, l5, 1.0f / 256.0f);
    twiddles(tw2, llo, 1.0f / 32.0f);
    twiddles_c(twc, bid, t & 4);
    const unsigned aW1 = L_FFT + 8u * (ra * EP + l5), aR1 = L_FFT + 8u * (ra * EP + k1l * KP + llo);
    const unsigned aLo = L_FFT + 8u * (ra * EP + kb), aHi = L_FFT + 8u * (rn * EP + kb);
    const unsigned a0 = kb ? aLo : aHi, a4 = kb ? aHi : aLo;
    __builtin_amdgcn_sched_barrier(0);
    cf s2 = {0.f, 0.f};
#pragma unroll
    for (int i = 0; i < 8; ++i) { v[i] = cf{bflo(raw[i]), bfhi(raw[i])}; s2 += v[i]; }
    const float mean = red32(s2.x + s2.y) * (1.0f / D_);
    STAMP(17);
    cf q2 = {0.f, 0.f};
#pragma unroll
    for (int i = 0; i < 8; ++i) { v[i] = v[i] - mean; q2 += v[i] * v[i]; }
    const float rinv = __builtin_amdgcn_rsqf(red32(q2.x + q2.y) * (1.0f / D_) + 1e-5f);
#pragma unroll
    for (int i = 0; i < 8; ++i) v[i] = v[i] * rinv;
    lds_st(L_ST8 + 8u * ra, cf{mean, rinv});
    dft8<0>(v); tw7<0>(v, twA);
#pragma unroll
    for (int i = 0; i < 8; ++i) lds_st(aW1 + 8u * KP * i, v[i]);
    fence_c();
#pragma unroll
    for (int i = 0; i < 8; ++i) v[i] = lds_ld(aR1 + 32u * i);
    dft8<0>(v); tw7<0>(v, tw2);
    quadT2(v);
    dft4x2<0>(v);
    fence_c();
#pragma unroll
    for (int i = 0; i < 8; ++i) { const int n = (i >> 2) + 2 * (i & 3); lds_st((n == 0 ? a0 : n == 4 ? a4 : n < 4 ? aLo : aHi) + 256u * n, v[i]); }
    fsync();
#pragma unroll
    for (int i = 0; i < 8; ++i) v[i] = lds_ld(L_FFT + 8u * (i * EP) + 8u * kt);
    if (t < 8) { const cf sv = lds_ld(L_ST8 + 8u * t); st8(ws, WS_STATS + 64u * bid + 8u * t, u32x2{__float_as_uint(sv.x), __float_as_uint(sv.y)}); }
    dft8<0>(v); tw7<0>(v, twc);
    if (w == 3) special_swap(v, lane);
    const unsigned o = WS_X + (unsigned)(t >> 3) * 131072u + (unsigned)bid * 64u + 8u * (t & 7);
#pragma unroll
    for (int i = 0; i < 8; ++i) st8(ws, o + 16384u * i, u32x2{__float_as_uint(v[i].x), __float_as_uint(v[i].y)});
    if (WAIT) { fft_end(4); tag = make_tag(epoch); *(volatile LAS u32x2*)ldsp(L_CTL + 128u) = tag; }
    STAMP(18);
    if (WAIT) { publish<8>(ws, C_EPUB, 12, WS_FA + 8u * bid, tag, lane); ctl_add_nw(C_FPUB); }
    STAMP(19);
  } else {
    twiddles(tw2, llo, 1.0f / 32.0f);
  }
  if (mode & PH_P2) {
    const int t = opaque(t_), lane = t & 63, hf = lane >> 5;
    const int k1l = (lane >> 2) & 7, llo = lane & 3, kb = k1l + 8 * llo;
    const int c = bid & 7, q = bid >> 3, eB = t & 7, p5 = t >> 3;
    const bool spq = q == 31 && (c >= 4 || c == 0);
    const int e = w + 4 * hf;
    const bool selfp = q == 31 && w == 3 && (c & 3) == 0;
    const bool sym = q == 31 && w == 3 && (c >= 5 || ((c & 3) == 0 && hf));
    const int ep = selfp ? e : e ^ 4;
    const int kcol = sym ? 0 : (hf ? 255 - 4 * q - w : 4 * q + 1 + w);
    const int dneg = c ? 255 : 256;
    const int kbp = (dneg - kb) & 31;
    const bool spec = (dneg - kb) == 256;
    cf twB[7];
    twiddles(twB, p5, 1.0f / 256.0f);
    constexpr float SC = 0.5f / (2048.0f * 256.0f);
    const cf cs = cis((float)kcol * (1.0f / 512.0f));
    const float ca = SC - SC * cs.y, cb = SC + SC * cs.y, cg = SC * cs.x;
    const unsigned aB = L_FFT + 8u * (eB * EP + p5), aR = L_FFT + 8u * (e * EP + k1l * KP + llo), aK = L_FFT + 8u * (e * EP + kb);
    const unsigned aQ = L_FFT + 8u * (ep * EP + kbp);
    STAMP(28);
    if (WAIT) {
      if (w == 0) {
        ctl_wait<1>(C_EPUB, 12u);
        wait_all(ws, WS_FA, 0, tag, lane);
        ctl_set(C_AREADY, 1u);
        ctl_set(C_PAUSE, 0u);
        if (bid == 0 && lane == 0) {
          const u64 e1 = (((u64)epoch.y << 32) | epoch.x) + 1ull;
          st8(ws, WS_EPOCH, u32x2{(unsigned)e1, (unsigned)(e1 >> 32)});
        }
      } else ctl_wait<0>(C_AREADY, 1u);
    }
    STAMP(20);
    const unsigned xo = WS_X + (unsigned)bid * 16384u + 8u * t;
#pragma unroll
    for (int i = 0; i < 8; ++i) { const u32x2 r = ld8(ws, xo + 2048u * i); v[i] = u2c(r.x, r.y); }
    const unsigned wo = WS_W + (unsigned)bid * 8192u + (unsigned)e * 1024u + 32u * kb, wpo = WS_W + (unsigned)bid * 8192u + (unsigned)ep * 1024u + 32u * kbp;
    const u32x4 wa0 = ld16(ws, wo), wa1 = ld16(ws, wo + 16), wb0 = ld16(ws, wpo), wb1 = ld16(ws, wpo + 16);
    u32x4 a0 = {0u, 0u, 0u, 0u}, a1 = a0, b0 = a0, b1 = a0;
    if (spq && w == 3) {
      const unsigned o1 = WS_W2 + (unsigned)(2 * c + hf) * 1024u + 32u * kb, o2 = WS_W2 + (unsigned)(2 * c + (ep >> 2)) * 1024u + 32u * kbp;
      a0 = ld16(ws, o1); a1 = ld16(ws, o1 + 16); b0 = ld16(ws, o2); b1 = ld16(ws, o2 + 16);
    }
    u32x2 sv = {0u, 0u};
    if ((mode & PH_G1) && w >= 2) sv = stat_ld(t);
    dft8<0>(v); tw7<0>(v, twB);
    STAMP(21);
#pragma unroll
    for (int i = 0; i < 8; ++i) lds_st(aB + 8u * KP * i, v[i]);
    if ((mode & PH_G1) && w >= 2) { *(LAS u32x2*)ldsp(L_STAT + 8u * (t - 128)) = sv; ctl_add_nw(C_MM); }
    fs += 4; ctl_add(C_FSYNC);
    cf al[8], be[8];
    {
      const unsigned w1[8] = {wa0.x, wa0.y, wa0.z, wa0.w, wa1.x, wa1.y, wa1.z, wa1.w};
      const unsigned wp[8] = {wb0.x, wb0.y, wb0.z, wb0.w, wb1.x, wb1.y, wb1.z, wb1.w};
      float W1x[8], W1y[8], W2x[8], W2y[8];
#pragma unroll
      for (int i = 0; i < 8; ++i) {
        const int n = (i >> 2) + 2 * (i & 3);
        const unsigned u1 = w1[n], u2 = spec ? wp[(8 - n) & 7] : wp[7 - n];
        W1x[i] = bflo(u1); W1y[i] = bfhi(u1); W2x[i] = bflo(u2); W2y[i] = bfhi(u2);
      }
      if (spq && w == 3) {
        const unsigned y1[8] = {a0.x, a0.y, a0.z, a0.w, a1.x, a1.y, a1.z, a1.w};
        const unsigned yp[8] = {b0.x, b0.y, b0.z, b0.w, b1.x, b1.y, b1.z, b1.w};
#pragma unroll
        for (int i = 0; i < 8; ++i) {
          const int n = (i >> 2) + 2 * (i & 3);
          const unsigned u1 = y1[n], u2 = spec ? yp[(8 - n) & 7] : yp[7 - n];
          if (sym) {
            W1x[i] = smul(sadd(W1x[i], W2x[i]), 0.5f); W1y[i] = smul(ssub(W1y[i], W2y[i]), 0.5f);
            W2x[i] = smul(sadd(bflo(u1), bflo(u2)), 0.5f); W2y[i] = smul(ssub(bfhi(u2), bfhi(u1)), 0.5f);
          }
        }
      }
      const float ncb = -cb, ncg = -cg;
#pragma unroll
      for (int i = 0; i < 8; ++i) {
        al[i] = cf{sfma(W1x[i], ca, smul(W2x[i], cb)), sfma(W1y[i], ca, smul(W2y[i], ncb))};
        be[i] = cf{smul(sadd(W2y[i], W1y[i]), ncg), smul(ssub(W1x[i], W2x[i]), cg)};
        asm volatile("" : "+v"(al[i]), "+v"(be[i]));
      }
    }
    ctl_wait<0>(C_FSYNC, fs);
#pragma unroll
    for (int i = 0; i < 8; ++i) v[i] = lds_ld(aR + 32u * i);
    dft8<0>(v); tw7<0>(v, tw2);
    quadT2(v);
    dft4x2<0>(v);
    fence_c();
#pragma unroll
    for (int i = 0; i < 8; ++i) lds_st(aK + 256u * ((i >> 2) + 2 * (i & 3)), v[i]);
    fence_c();
    cf qv[8];
    {
      const unsigned aQs = aQ + (spec ? 256u : 0u);
#pragma unroll
      for (int i = 0; i < 8; ++i) {
        const int n = (i >> 2) + 2 * (i & 3);
        if (n == 0) qv[i] = lds_ld(spec ? aQ : aQ + 256u * 7);
        else qv[i] = lds_ld(aQs + 256u * (7 - n));
      }
    }
    filt4(v, qv, al, be); filt4(v + 4, qv + 4, al + 4, be + 4);
    dft4x2<1>(v);
    quadT2(v);
    tw7<1>(v, tw2); dft8<1>(v);
    fence_c();
#pragma unroll
    for (int i = 0; i < 8; ++i) lds_st(aR + 32u * i, v[i]);
    fsync();
#pragma unroll
    for (int i = 0; i < 8; ++i) v[i] = lds_ld(aB + 8u * KP * i);
    tw7<1>(v, twB); dft8<1>(v);
    const unsigned zo = WS_Z + (unsigned)p5 * 16384u + (unsigned)c * 2048u + (unsigned)q * 64u + 8u * eB;
#pragma unroll
    for (int i = 0; i < 8; ++i) st8(ws, zo + 524288u * i, u32x2{__float_as_uint(v[i].x), __float_as_uint(v[i].y)});
    if (WAIT) fft_end(8);
    STAMP(22);
    if (WAIT) publish<8>(ws, C_FPUB, 8, WS_FC + 8u * bid, tag, lane);
    STAMP(23);
    if ((mode & PH_G1) && w >= 2) gather_cs(lane);
  }
  if (mode & PH_P3) {
    const int t = opaque(t_), lane = t & 63, hq = lane >> 5;
    const int l5 = lane & 31, k1l = (lane >> 2) & 7, llo = lane & 3, kb = k1l + 8 * llo;
    const int k0 = 4 * (t >> 3) + 1 + (t & 3);
    const int kt = (t & 4) ? (t == 255 ? 0 : 256 - k0) : k0;
    cf twA[7], twc[7];
    twiddles(twA, l5, 1.0f / 256.0f);
    twiddles_c(twc, bid, t & 4);
    const int ra = hq ? (w ? 8 - w : 4) : w, rn = (8 - ra) & 7;
    const unsigned aW1 = L_FFT + 8u * (ra * EP + l5), aR1 = L_FFT + 8u * (ra * EP + k1l * KP + llo);
    const unsigned aLo = L_FFT + 8u * (ra * EP + kb), aHi = L_FFT + 8u * (rn * EP + kb);
    const unsigned a0 = kb ? aLo : aHi, a4 = kb ? aHi : aLo;
    if (WAIT) {
      if (w == 0) { ctl_wait<1>(C_FPUB, 8u); wait_all(ws, WS_FC, 2, tag, lane); ctl_set(C_CREADY, 1u); ctl_set(C_PAUSE, 0u); }
      else ctl_wait<0>(C_CREADY, 1u);
    }
    STAMP(24);
    const unsigned o = WS_Z + (unsigned)bid * 16384u + 8u * t;
#pragma unroll
    for (int i = 0; i < 8; ++i) { const u32x2 r = ld8(ws, o + 2048u * i); v[i] = u2c(r.x, r.y); }
    if (w == 3) special_swap(v, lane);
    tw7<1>(v, twc); dft8<1>(v);
    STAMP(25);
#pragma unroll
    for (int i = 0; i < 8; ++i) lds_st(L_FFT + 8u * (i * EP) + 8u * kt, v[i]);
    fsync();
#pragma unroll
    for (int i = 0; i < 8; ++i) { const int n = (i >> 2) + 2 * (i & 3); v[i] = lds_ld((n == 0 ? a0 : n == 4 ? a4 : n < 4 ? aLo : aHi) + 256u * n); }
    dft4x2<1>(v);
    quadT2(v);
    tw7<1>(v, tw2); dft8<1>(v);
    fence_c();
#pragma unroll
    for (int i = 0; i < 8; ++i) lds_st(aR1 + 32u * i, v[i]);
    fence_c();
#pragma unroll
    for (int i = 0; i < 8; ++i) v[i] = lds_ld(aW1 + 8u * KP * i);
    ctl_add(C_FDONE);
    tw7<1>(v, twA); dft8<1>(v);
    const unsigned so = WS_S + (unsigned)(bid + 256 * ra) * 1024u + 4u * l5;
#pragma unroll
    for (int i = 0; i < 8; ++i) st4(ws, so + 128u * i, pk2(v[i].x, v[i].y));
    if (WAIT) fft_end(12);
    STAMP(26);
    if (WAIT) publish<8>(ws, C_FPUB, 12, WS_FD + 8u * bid, tag, lane);
    STAMP(27);
  }
}

__host__ __device__ constexpr unsigned wd_addr0(int u) {
  return u < 8 ? L_SPARE + 2048u * u : u < 28 ? 81920u + 2048u * (u - 8) : u < 48 ? 2048u * (u - 28) : 40960u + 2048u * (u - 48);
}
__host__ __device__ constexpr unsigned wd_addr(int u) { return wd_addr0(u >= 68 ? u - 68 : u); }
DI bf16x8 tr2(unsigned a0, unsigned a1) {
  const s16x4 lo = __builtin_amdgcn_ds_read_tr16_b64_v4i16((LAS s16x4*)ldsp(a0));
  const s16x4 hi = __builtin_amdgcn_ds_read_tr16_b64_v4i16((LAS s16x4*)ldsp(a1));
  return __builtin_shufflevector(lo, hi, 0, 1, 2, 3, 4, 5, 6, 7);
}
DI float dot1(unsigned u, float acc) {
  const unsigned o = 0x3F803F80u;
  return __builtin_amdgcn_fdot2_f32_bf16(__builtin_bit_cast(bf16x2, u), __builtin_bit_cast(bf16x2, o), acc, false);
}
DI float dot_ones(bf16x8 f, float acc) {
  const u32x4 u = __builtin_bit_cast(u32x4, f);
  const unsigned a = u.x, b = u.y, c = u.z, d = u.w;
  return dot1(d, dot1(c, dot1(b, dot1(a, acc))));
}
#define MFMA(a, b, c) __builtin_amdgcn_mfma_f32_32x32x16_bf16((a), (b), (c), 0, 0, 0)
DI unsigned wfrag_off(int lane, int fg) {
  const int g4 = lane >> 4, q4 = (lane >> 2) & 3, pp = lane & 3, mat = g4 & 1;
  return 16384u + (unsigned)mat * 12288u + (unsigned)(2 * (g4 >> 1)) * 768u + (unsigned)q4 * 64u + (unsigned)(fg ^ mat) * 32u + 8u * pp;
}

template <int mode> DI void load_role(const Args& A, int L, int lane) {
  const int bid = blockIdx.x, l = L & 1, pr = L >> 1;
  constexpr bool WAIT = mode & PH_WAIT;
  const Tile T = tile_of(bid);
  const int g4 = lane >> 4, q4 = (lane >> 2) & 3, pp = lane & 3;
  auto pub = [&](unsigned n) { ctl_set(C_FULL0 + L, n); };
  if (mode & PH_G1) {
    unsigned xv[2], wv[3]; u64 wm[3];
#pragma unroll
    for (int i = 0; i < 2; ++i) { const int row = i * 8 + (lane >> 3); xv[i] = (unsigned)row * 1024u + 16u * ((lane & 7) ^ ((row >> 1) & 7)); }
#pragma unroll
    for (int i = 0; i < 3; ++i) {
      const int cell = 4 * i + g4, a = cell / 3, pair = cell - 3 * a;
      wv[i] = (unsigned)(4 * a + q4) * 2752u + (unsigned)pair * 64u + 16u * (pp ^ (2 * l));
      wm[i] = __ballot(pair < T.np);
    }
    const char* xb = (const char*)A.x + (size_t)(T.mt * 128 + l * 64) * 1024;
    const char* wb = (const char*)(l ? A.wu : A.wg) + T.start * 64;
    if (mode & PH_P0) { __builtin_amdgcn_s_sleep(LDELAY); if (pr) __builtin_amdgcn_s_sleep(LDELAY2); }
#pragma nounroll
    for (int s = pr; s < 8; s += 2) {
      if (s >= 3) ctl_wait<0>(C_FR0 + s % 3, 8u * (s / 3));
      const int sr = (s + T.rot1) & 7;
      const unsigned slot = (unsigned)(s % 3) * L_SLOT;
      STAMPL(32 + s);
#pragma unroll
      for (int g = 0; g < 4; ++g) {
#pragma unroll
        for (int i = 0; i < 2; ++i) glds(xv[i], xb + g * 16384 + sr * 128, slot + (unsigned)(l * 8 + 2 * g + i) * 1024u);
#pragma unroll
        for (int i = 0; i < 3; ++i)
          glds_m(wv[i], wb + (size_t)(sr * 64 + g * 16) * 2752, slot + 16384u + (unsigned)l * 12288u + (unsigned)(3 * g + i) * 1024u, wm[i]);
        if (g == 1 && s == 2) { wait_vm<10>(); pub(1); }
      }
      STAMPL(40 + s);
      if (s != 0) { wait_vm<0>(); pub(s + 1); }
    }
  }
  if (!(mode & PH_G2)) return;
  if (pr == 0) {
    const unsigned wdv = (unsigned)(lane >> 3) * 1024u + 16u * ((lane & 7) ^ (((lane >> 4) & 1) << 2));
    const char* wdb = (const char*)A.wd + T.nt2 * 128 + l * 8192;
    auto wd_units = [&](int u0, int u1, unsigned dst) {
#pragma nounroll
      for (int u = u0; u < u1; ++u) { const int ur = u + T.rot2 >= 86 ? u + T.rot2 - 86 : u + T.rot2; glds(wdv, wdb + (size_t)ur * 16384, dst + 2048u * (u - u0) + 1024u * l); }
    };
    wd_units(0, 8, L_SPARE);
    if (mode & PH_G1) ctl_wait<1>(C_FR0 + 2, 16u);
    wd_units(8, 28, 81920u);
    if (mode & PH_G1) ctl_wait<1>(C_FR0, 24u);
    wd_units(28, 48, 0u);
    wait_vm<20>(); pub(10); STAMPL(48);
    if (mode & PH_G1) ctl_wait<1>(C_FR0 + 1, 24u);
    STAMPL(49);
    wd_units(48, 68, 40960u);
    wait_vm<0>(); pub(12); STAMPL(50);
    ctl_wait<1>(C_FREE2A, 8u);
    STAMPL(51);
    wd_units(68, 76, L_SPARE);
    ctl_wait<0>(C_FREE2, 8u);
    wd_units(76, 86, 81920u);
    wait_vm<10>(); pub(13);
    wait_vm<0>(); pub(14); STAMPL(52);
  } else {
    const rsrc_t ws = __builtin_amdgcn_make_buffer_rsrc(A.ws, 0, WS_END, 0x00020000);
    u32x2 tag = {0u, 0u};
    if (WAIT) { ctl_wait<2>(C_FPUB, 1u); tag = *(volatile LAS u32x2*)ldsp(L_CTL + 128u); tag.x = __builtin_amdgcn_readfirstlane(tag.x); tag.y = __builtin_amdgcn_readfirstlane(tag.y); }
    const int tok0 = T.mt2 * 64 + l * 32 + (lane >> 3);
    u32x4 xq[4], sq[4];
#pragma unroll
    for (int i = 0; i < 4; ++i) xq[i] = *(const u32x4*)(A.x + (size_t)(tok0 + 8 * i) * D_ + T.nt2 * 64 + (lane & 7) * 8);
    if (WAIT) {
      if (l == 0) { ctl_wait<2>(C_GPUB, 8u); poll_flags(ws, WS_FB + 128u * (T.mt2 >> 1), 16, tag, lane); ctl_set(C_HREADY, 1u); }
      ctl_wait<2>(C_FPUB, 12u);
      poll_flags(ws, WS_FD + 2048u * (bid & 7) + 8u * ((T.mt2 * 64 + l * 32) & 255), 32, tag, lane);
    }
#pragma unroll
    for (int i = 0; i < 4; ++i) sq[i] = ld16(ws, WS_S + (unsigned)(tok0 + 8 * i) * 1024u + (unsigned)T.nt2 * 128u + 16u * (lane & 7));
    if (mode & PH_P3) ctl_wait<1>(C_FDONE, 4u);
    const unsigned lo = L_FFT + 4096u * l + 16u * lane;
#pragma unroll
    for (int i = 0; i < 4; ++i) { *(LAS u32x4*)ldsp(lo + 1024u * i) = xq[i]; *(LAS u32x4*)ldsp(lo + 8192u + 1024u * i) = sq[i]; }
    ctl_add(C_SXREADY);
  }
}

#define SB() __builtin_amdgcn_sched_barrier(0)
DI void pre_finish(const Args& A, rsrc_t ws, int w, int lane) {
  const int bid = blockIdx.x;
  if (bid < 176 && w == 0) {
    ctl_wait<0>(C_CSW, 8u);
    cf cs = lds_ld(L_CS + 8u * lane);
#pragma unroll
    for (int z = 1; z < 8; ++z) { const cf b = lds_ld(L_CS + 512u * z + 8u * lane); cs = cf{sadd(cs.x, b.x), sadd(cs.y, b.y)}; }
    st8(ws, WS_CS + 4u * ((bid / 22) * 2816 + (bid % 22) * 128 + 2 * lane), u32x2{__float_as_uint(cs.x), __float_as_uint(cs.y)});
  }
}
template <int NP> DI void g1_loop(const Args& A, f32x16 (&acc)[3], unsigned xa, unsigned wa) {
  unsigned slot = 0;
  bf16x8 xf[5], wf[5][NP];
  full_wait<1>(1, 0);
  STAMP(1);
  xf[0] = *(LAS bf16x8*)ldsp(xa);
#pragma unroll
  for (int p = 0; p < NP; ++p) wf[0][p] = tr2(wa + p * 256u, wa + 768u + p * 256u);
#pragma nounroll
  for (int s = 0; s < 8; ++s) {
    if (s == 1) STAMP(2);
    if (s == 4) { STAMP(3); STAMPC(8); }
    const unsigned nslot = slot == 2 * L_SLOT ? 0 : slot + L_SLOT;
    unsigned pk0 = 0, pk1 = 0, pk2_ = 0;
    unsigned wsl = wa + slot, xsl = xa + slot;
#pragma unroll
    for (int ks = 0; ks < 4; ++ks) {
      if (ks == 3) {
        const unsigned m = pk0 < pk1 ? pk0 : pk1, need = s < 7 ? (unsigned)s + 2u : 0u;
        if (__builtin_amdgcn_readfirstlane(m < pk2_ ? m : pk2_) < need) full_wait<1>(need, (s + 1) & 1);
        const unsigned sl = s < 7 ? nslot : slot;
        wsl = wa + sl; xsl = xa + sl;
      }
      const int kn = (ks + 1) & 3;
      SB();
      acc[0] = MFMA(wf[ks][0], xf[ks], acc[0]);
      SB();
      xf[ks + 1] = *(LAS bf16x8*)ldsp(xsl ^ (32u * kn));
      wf[ks + 1][0] = tr2(wsl + 3072u * kn, wsl + 3072u * kn + 768u);
      SB();
      acc[1] = MFMA(wf[ks][1], xf[ks], acc[1]);
      SB();
      wf[ks + 1][1] = tr2(wsl + 3072u * kn + 256u, wsl + 3072u * kn + 1024u);
      if (NP == 3) {
        SB();
        acc[2] = MFMA(wf[ks][NP - 1], xf[ks], acc[2]);
        SB();
        wf[ks + 1][NP - 1] = tr2(wsl + 3072u * kn + 512u, wsl + 3072u * kn + 1280u);
      }
      if (ks == 1) {
        const unsigned pa = L_CTL + 8u * ((s + 1) & 1);
        pk0 = *(volatile LAS unsigned*)ldsp(pa); pk1 = *(volatile LAS unsigned*)ldsp(pa + 4); pk2_ = *(volatile LAS unsigned*)ldsp(L_CTL + 4 * C_PAUSE);
      }
      if (ks == 3) { if (s == 7) wait_lgkm(); ctl_add_nw(C_FR0 + s % 3); }
      SB();
    }
    xf[0] = xf[4];
#pragma unroll
    for (int p = 0; p < NP; ++p) wf[0][p] = wf[4][p];
    slot = nslot;
  }
}
template <int mode> DI void gemm_role(const Args& A, int t, rsrc_t ws, u32x2 epoch) {
  const int lane = t & 63, w = __builtin_amdgcn_readfirstlane(t >> 6);
  constexpr bool WAIT = mode & PH_WAIT;
  const Tile T = tile_of(blockIdx.x);
  const int r = lane & 31, h = lane >> 5;
  if (mode & PH_P0) {
    const int bid = blockIdx.x, rowc = bid & 7, kbp = (bid >> 3) & 15, side = bid >> 7;
    const int rr = t & 15, q = t >> 4, c = side ? (8 - rowc) & 7 : rowc;
    const int ro = (rowc + 8 * (2 * kbp + (rr >> 3) + 32 * (rr & 7))) * DF_;
    const int co = ro + (side ? 252 - 4 * q : 4 * q + 1);
    const U8 a = *(const U8*)(A.wr + co), b = *(const U8*)(A.wi + co);
    const int cg = bid % 22, ks8 = bid / 22;
    if (bid < 176) {
      const int cp = (cg % 11) * 64 + lane;
      const unsigned* wp = (const unsigned*)(cg >= 11 ? A.wu : A.wg) + (size_t)(ks8 * 64 + w * 8) * 688 + (cp < 688 ? cp : 0);
      unsigned cw[8];
#pragma unroll
      for (int z = 0; z < 8; ++z) cw[z] = wp[z * 688];
      cf cs = {0.f, 0.f};
#pragma unroll
      for (int z = 0; z < 8; ++z) cs += cf{bflo(cw[z]), bfhi(cw[z])};
      lds_st(L_CS + 512u * w + 8u * lane, cs);
      ctl_add(C_CSW);
    }
    unsigned wd[4] = {__builtin_amdgcn_perm(b.a, a.a, 0x05040100u), __builtin_amdgcn_perm(b.a, a.a, 0x07060302u),
                      __builtin_amdgcn_perm(b.b, a.b, 0x05040100u), __builtin_amdgcn_perm(b.b, a.b, 0x07060302u)};
    if (q == 31 && (c >= 5 || ((c & 3) == 0 && side))) {
      const unsigned z0 = (unsigned)A.wr[ro] | ((unsigned)A.wi[ro] << 16), z1 = (unsigned)A.wr[ro + 256] | ((unsigned)A.wi[ro + 256] << 16);
      if (side) wd[0] = z0; else wd[3] = z0;
      st4(ws, WS_W2 + (unsigned)(2 * c + side) * 1024u + 64u * kbp + 4u * rr, z1);
    }
    const unsigned o = WS_W + (unsigned)(q * 8 + c) * 8192u + (unsigned)side * 4096u + 64u * kbp + 4u * rr;
#pragma unroll
    for (int z = 0; z < 4; ++z) st4(ws, o + 1024u * (side ? 3 - z : z), wd[z]);
    if (!(mode & PH_G1)) pre_finish(A, ws, w, lane);
  }
  if (mode & PH_G1) {
    const int tg = w & 3, fg = w >> 2;
    const int xrow = tg * 32 + r;
    const unsigned xa = (unsigned)xrow * 128u + 16u * (h ^ ((xrow >> 1) & 7));
    const unsigned wa = wfrag_off(lane, fg);
    f32x16 acc[3];
#pragma unroll
    for (int p = 0; p < 3; ++p)
#pragma unroll
      for (int i = 0; i < 16; ++i) acc[p][i] = 0.f;
    if (mode & PH_P0) { pre_finish(A, ws, w, lane); if (WAIT) publish<8>(ws, C_EPUB, 12, WS_FA + 8u * blockIdx.x, make_tag(epoch), lane); }
    STAMP(0);
    if (T.np == 3) g1_loop<3>(A, acc, xa, wa); else g1_loop<2>(A, acc, xa, wa);
    STAMP(4);
    STAMPC(6);
    ctl_wait<1>(C_MM, 2u);
    ctl_wait<1>(C_CSR, 2u);
    const cf st = lds_ld(L_STAT + 8u * xrow);
    const float m0 = rbf(-st.x), mr = -st.x - m0, m1 = rbf(mr), m2 = mr - m1;
    const u32x4 bq = {pk2(m0, m0), pk2(m0, m1), pk2(m1, m2), 0u};
    const bf16x8 bfrag = __builtin_bit_cast(bf16x8, bq);
    constexpr float K0 = -2.0f * 0.7978845608028654f * 1.4426950408889634f, K1 = K0 * 0.044715f;
    const float k0 = K0 * st.y, k1 = K1 * st.y * st.y * st.y, ir2 = __builtin_amdgcn_rcpf(st.y * st.y);
#pragma unroll
    for (int p = 0; p < 3; ++p) {
      if (p < T.np) {
        const float co = *(LAS float*)ldsp(L_CS + 384u * (r >> 4) + 4u * (32 * p + 16 * fg + (r & 15)));
        const float ct = h ? 0.f : co;
        const float c0 = rbf(ct), r1 = ct - c0, c1 = rbf(r1), c2 = r1 - c1;
        const u32x4 aq = {pk2(c0, c1), pk2(c2, c0), pk2(c1, c0), 0u};
        const f32x16 a = MFMA(__builtin_bit_cast(bf16x8, aq), bfrag, acc[p]);
        unsigned hv[4];
#pragma unroll
        for (int i = 0; i < 4; ++i) {
          const cf g = {a[2 * i], a[2 * i + 1]}, u = {a[8 + 2 * i], a[9 + 2 * i]};
          const cf z = g * (g * g * k1 + k0);
          const cf d = cf{__builtin_amdgcn_exp2f(z.x), __builtin_amdgcn_exp2f(z.y)} * ir2 + ir2;
          const cf hh = (g * u) * cf{__builtin_amdgcn_rcpf(d.x), __builtin_amdgcn_rcpf(d.y)};
          hv[i] = pk2(hh.x, hh.y);
        }
        const int unit = 2 * (T.start + p) + fg;
        st16(ws, WS_H + (unsigned)((T.mt * 4 + tg) * 86 + unit) * 1024u + 16u * lane, u32x4{hv[0], hv[1], hv[2], hv[3]});
      }
    }
    STAMP(5);
    if (WAIT) publish<1>(ws, C_GPUB, 8, WS_FB + 8u * (T.mt * 16 + T.nt), make_tag(epoch), lane);
  }
  if (mode & PH_G2) {
    const int th = w & 1, kq = w >> 1;
    const int g4 = lane >> 4, q4 = (lane >> 2) & 3, pp = lane & 3;
    const int dsub = g4 & 1, hh = g4 >> 1;
    unsigned wa2[2];
#pragma unroll
    for (int dh = 0; dh < 2; ++dh)
      wa2[dh] = (unsigned)kq * 2048u + (unsigned)(4 * hh + q4) * 128u + (unsigned)(dh ^ (q4 >> 1)) * 64u + (unsigned)dsub * 32u + 8u * pp;
    const unsigned wa3[2] = {wa2[0] + 81920u, wa2[1] + 81920u};
    f32x16 acc[2];
#pragma unroll
    for (int p = 0; p < 2; ++p)
#pragma unroll
      for (int i = 0; i < 16; ++i) acc[p][i] = 0.f;
    STAMP(7);
    if (WAIT) ctl_wait<1>(C_HREADY, 1u);
    STAMP(9);
    const unsigned ho = WS_H + (unsigned)((2 * T.mt2 + th) * 86) * 1024u + 16u * lane;
    auto hoff = [&](int i) { const int u = 4 * i + kq + T.rot2; return (unsigned)(u >= 86 ? u - 86 : u) * 1024u; };
    auto hld = [&](int i) { return __builtin_amdgcn_raw_buffer_load_b128(ws, ho, hoff(i == 21 && kq >= 2 ? 20 : i), 16); };
    constexpr int WIN = HWIN;
    u32x4 hf[22];
#pragma unroll
    for (int i = 0; i < WIN; ++i) hf[i] = hld(i);
    bf16x8 wq[2][2];
#define WLD(i, dh) do { const unsigned ad = wd_addr(4 * (i)); \
      const unsigned bb = ad >= 81920u ? wa3[dh] : wa2[dh], so = ad >= 81920u ? ad - 81920u : ad; \
      wq[(i) & 1][dh] = tr2(bb + so, bb + so + 1024u); } while (0)
    full_wait<0>(10, 0);
    WLD(0, 0); WLD(0, 1);
#pragma unroll
    for (int i = 0; i < 22; ++i) {
      if (i == 1) STAMP(10);
      if (i == 17) STAMP(11);
      if (i == 3) full_wait<0>(10, 0);
      if (i == 6 || i == 11) full_wait<0>(12, 0);
      if (i == 16) full_wait<0>(13, 0);
      if (i == 18) full_wait<0>(14, 0);
      {
        if (i == 21 && kq >= 2) hf[i] = u32x4{0u, 0u, 0u, 0u};
        const bf16x8 hb = __builtin_bit_cast(bf16x8, hf[i]);
        SB();
        acc[0] = MFMA(wq[i & 1][0], hb, acc[0]);
        asm volatile("" : "+v"(acc[0]));
        SB();
        if (i < 21) WLD(i + 1, 0);
        if (i + WIN < 22) hf[i + WIN] = hld(i + WIN);
        SB();
        acc[1] = MFMA(wq[i & 1][1], hb, acc[1]);
        asm volatile("" : "+v"(acc[1]));
        SB();
        if (i < 21) WLD(i + 1, 1);
        if (i == 1) ctl_add_nw(C_FREE2A);
        if (i == 4) ctl_add_nw(C_FREE2);
        SB();
      }
    }
    STAMP(12);
    ctl_add(C_RED);
    ctl_wait<0>(C_RED, 8u);
    const unsigned pw = (unsigned)kq * 17408u + (unsigned)(th * 32 + r) * 272u + 16u * h;
#pragma unroll
    for (int dh = 0; dh < 2; ++dh)
#pragma unroll
      for (int g = 0; g < 4; ++g)
        *(LAS f32x4*)ldsp(pw + dh * 128u + g * 32u) = f32x4{acc[dh][4 * g], acc[dh][4 * g + 1], acc[dh][4 * g + 2], acc[dh][4 * g + 3]};
    ctl_add(C_RED);
    ctl_wait<0>(C_RED, 16u);
    const int tok = t >> 3, d8 = t & 7;
    const unsigned pr = (unsigned)tok * 272u + 32u * d8;
    f32x4 o0 = *(LAS f32x4*)ldsp(pr), o1 = *(LAS f32x4*)ldsp(pr + 16);
#pragma unroll
    for (int z = 1; z < 4; ++z) { o0 += *(LAS f32x4*)ldsp(pr + z * 17408u); o1 += *(LAS f32x4*)ldsp(pr + z * 17408u + 16); }
    const float ov[8] = {o0.x, o0.y, o0.z, o0.w, o1.x, o1.y, o1.z, o1.w};
    float gs[8];
#pragma unroll
    for (int i = 0; i < 8; ++i) gs[i] = ov[i] * __builtin_amdgcn_rcpf(1.0f + __builtin_amdgcn_exp2f(-1.4426950408889634f * ov[i]));
    STAMP(13);
    ctl_wait<1>(C_SXREADY, 2u);
    STAMP(14);
    const u32x4 xq = *(LAS u32x4*)ldsp(L_FFT + 16u * t), sq = *(LAS u32x4*)ldsp(L_FFT + 8192u + 16u * t);
    u32x4 res;
#pragma unroll
    for (int i = 0; i < 4; ++i) {
      const unsigned sx = sq[i], xx = xq[i];
      res[i] = pk2(bflo(sx) * gs[2 * i] + bflo(xx), bfhi(sx) * gs[2 * i + 1] + bfhi(xx));
    }
    *(u32x4*)(A.out + (size_t)(T.mt2 * 64 + tok) * D_ + T.nt2 * 64 + d8 * 8) = res;
    STAMP(15);
  }
}

template <int MODE>
__global__ __launch_bounds__(1024, 4) void fused_kernel(Args A) {
  u64 pc;
  asm volatile("s_getpc_b64 %0" : "=s"(pc));
  const int tid = threadIdx.x, wv = __builtin_amdgcn_readfirstlane(tid >> 6);
  if (wv == 15) { *(volatile LAS unsigned*)ldsp(L_CTL + 4u * (tid & 63)) = ((tid & 63) == C_PAUSE && !(MODE & PH_WAIT)) ? ~0u : 0u; wait_lgkm(); }
  if (wv == 14) { STAMP_INIT(32, 24); wait_lgkm(); }
  __builtin_amdgcn_s_barrier();
  if (wv >= 4 && wv < 8) {
    __builtin_amdgcn_s_setprio(2);
    load_role<MODE>(A, wv - 4, tid & 63);
    if (wv == 4) STAMP_DUMP(32, 21);
    return;
  }
  const rsrc_t ws = __builtin_amdgcn_make_buffer_rsrc(A.ws, 0, WS_END, 0x00020000);
  u32x2 epoch = {0u, 0u};
  if (MODE & PH_WAIT) epoch = ld8(ws, WS_EPOCH);
  if (wv >= 8) {
    if (wv == 8) STAMP_INIT(0, 16);
    gemm_role<MODE>(A, tid - 512, ws, epoch);
    if (wv == 8) STAMP_DUMP(0, 16);
  } else {
    __builtin_amdgcn_s_setprio(3);
    if (wv == 0) STAMP_INIT(16, 16);
    fft_role<MODE>(A, tid, ws, epoch);
    if (wv == 0) STAMP_DUMP(16, 16);
  }
}

extern "C" void kernel_launch(void* const* d_in, const int* in_sizes, int n_in, void* d_out, int out_size, void* d_ws,
                              size_t ws_size, hipStream_t stream) {
  Args A;
  A.x = (const uint16_t*)d_in[0]; A.wr = (const uint16_t*)d_in[1]; A.wi = (const uint16_t*)d_in[2];
  A.wg = (const uint16_t*)d_in[4]; A.wu = (const uint16_t*)d_in[5]; A.wd = (const uint16_t*)d_in[6];
  A.out = (uint16_t*)d_out; A.ws = (char*)d_ws;
#define LAUNCH(M) do { (void)hipFuncSetAttribute((const void*)fused_kernel<M>, hipFuncAttributeMaxDynamicSharedMemorySize, L_TOTAL); \
    fused_kernel<M><<<256, 1024, L_TOTAL, stream>>>(A); } while (0)
  LAUNCH(PH_P0 | PH_G1 | PH_P2 | PH_P3 | PH_G2 | PH_WAIT);
}
```

```cpp
#include <hip/hip_runtime.h>
#include <stdint.h>

#define LTHR 6
#define LTHR2 12
#define GSLEEP 1
#define BSLEEP 4
#define LDELAY 8
#define LDELAY2 16
#define SC0 0
#define SC2 1
#define SC3 1
#define AGG 255
#define HWIN 6

#define DI __device__ __forceinline__
#define LAS __attribute__((address_space(3)))
typedef __attribute__((ext_vector_type(2))) float cf;
typedef __attribute__((ext_vector_type(4))) float f32x4;
typedef __attribute__((ext_vector_type(16))) float f32x16;
typedef __attribute__((ext_vector_type(8))) short bf16x8;
typedef __attribute__((ext_vector_type(4))) short s16x4;
typedef __attribute__((ext_vector_type(2))) __bf16 bf16x2;
typedef __attribute__((ext_vector_type(4))) unsigned u32x4;
typedef __attribute__((ext_vector_type(2))) unsigned u32x2;
typedef unsigned long long u64;

constexpr int D_ = 512, DF_ = 257;
constexpr unsigned WS_EPOCH = 0;
constexpr unsigned WS_FB = 4096;
constexpr unsigned WS_FA = 131072, WS_FC = 147456, WS_FD = 163840, WS_FE = 180224;
constexpr unsigned WS_GO = 8192;
constexpr unsigned WS_CNT = 16384;
constexpr unsigned WS_CS = 262144;
constexpr unsigned WS_STATS = 32768;
constexpr unsigned WS_W2 = 65536;
constexpr unsigned WS_X = 1u << 20;
constexpr unsigned WS_Z = 5u << 20;
constexpr unsigned WS_W = 9u << 20;
constexpr unsigned WS_S = 11u << 20;
constexpr unsigned WS_H = 13u << 20;
constexpr unsigned WS_END = 20u << 20;
constexpr unsigned L_SLOT = 40960;
constexpr unsigned L_SPARE = 122880;
constexpr unsigned L_CS = 139264;
constexpr unsigned L_FFT = 143360;
constexpr unsigned L_STAT = L_FFT + 18688;
constexpr unsigned L_ST8 = L_STAT + 1024;
constexpr unsigned L_CTL = L_ST8 + 64;
constexpr unsigned L_TOTAL = L_CTL + 256;
static_assert(L_TOTAL <= 163840, "LDS");
constexpr int EP = 292, KP = 36;
enum { C_FULL0 = 0  , C_PAUSE = 4, C_FSYNC = 5, C_FPUB = 6, C_GPUB = 7, C_AREADY = 8, C_CREADY = 9, C_HREADY = 10,
       C_SXREADY = 11, C_RED = 12, C_EPUB = 13, C_FREE2 = 14, C_MM = 15, C_EREADY = 16, C_FDONE = 17, C_FR0 = 18  ,
       C_FEND = 21, C_CSW = 22, C_FREE2A = 23, C_CSR = 24, C_G1D = 25 };
enum { PH_P0 = 1, PH_G1 = 2, PH_P2 = 4, PH_P3 = 8, PH_G2 = 16, PH_WAIT = 32 };
constexpr unsigned SPIN_CAP = 1u << 16;

struct Args {
  const uint16_t *x, *wr, *wi, *wg, *wu, *wd;
  uint16_t* out;
  char* ws;
};

DI float bflo(unsigned u) { return __uint_as_float(u << 16); }
DI float bfhi(unsigned u) { return __uint_as_float(u & 0xffff0000u); }
DI unsigned pk2(float a, float b) { cf v = {a, b}; bf16x2 c = __builtin_convertvector(v, bf16x2); return __builtin_bit_cast(unsigned, c); }
DI float rbf(float a) { return bflo(pk2(a, a)); }
DI cf cis(float rev) { return cf{__builtin_amdgcn_cosf(rev), __builtin_amdgcn_sinf(rev)}; }

#define SUBM "neg_lo:[0,1] neg_hi:[0,1]"
#define SWP "op_sel:[0,1] op_sel_hi:[1,0]"
#define ADDROT_F SWP " neg_hi:[0,1]"
#define SUBROT_F SWP " neg_lo:[0,1]"
#define ADDROT_I SWP " neg_lo:[0,1]"
#define SUBROT_I SWP " neg_hi:[0,1]"
#define DFT8_BODY(AR, SR, RSUB, FAR, FSR)                                                                          \
  "v_pk_add_f32 %8, %0, %4\n\tv_pk_add_f32 %12, %0, %4 " SUBM "\n\t"                                               \
  "v_pk_add_f32 %9, %1, %5\n\tv_pk_add_f32 %13, %1, %5 " SUBM "\n\t"                                               \
  "v_pk_add_f32 %10, %2, %6\n\tv_pk_add_f32 %14, %2, %6 " SUBM "\n\t"                                              \
  "v_pk_add_f32 %11, %3, %7\n\tv_pk_add_f32 %15, %3, %7 " SUBM "\n\t"                                              \
  "v_pk_add_f32 %1, %13, %13 " AR "\n\t"                                                                           \
  "v_pk_add_f32 %0, %8, %10\n\tv_pk_add_f32 %2, %8, %10 " SUBM "\n\t"                                              \
  "v_pk_add_f32 %3, %15, %15 " RSUB "\n\t"                                                                         \
  "v_pk_add_f32 %4, %9, %11\n\tv_pk_add_f32 %6, %9, %11 " SUBM "\n\t"                                              \
  "v_pk_add_f32 %5, %12, %14 " AR "\n\tv_pk_add_f32 %7, %12, %14 " SR "\n\t"                                       \
  "v_pk_add_f32 %8, %1, %3\n\tv_pk_add_f32 %9, %1, %3 " SUBM "\n\t"                                                \
  "v_pk_add_f32 %12, %0, %4\n\tv_pk_add_f32 %13, %0, %4 " SUBM "\n\t"                                              \
  "v_pk_add_f32 %14, %2, %6 " AR "\n\tv_pk_add_f32 %15, %2, %6 " SR "\n\t"                                         \
  "v_pk_fma_f32 %10, %8, %16, %5\n\tv_pk_fma_f32 %11, %9, %16, %7 " FAR "\n\t"                                     \
  "v_pk_fma_f32 %8, %8, %16, %5 neg_lo:[1,0,0] neg_hi:[1,0,0]\n\tv_pk_fma_f32 %9, %9, %16, %7 " FSR
#define FSWP "op_sel:[1,0,0] op_sel_hi:[0,1,1]"
template <int INV> DI void dft8(cf (&v)[8]) {
  const u64 R = 0x3F3504F33F3504F3ull;
  cf t0, t1, t2, t3, t4, t5, t6, t7;
  if (!INV)
    asm(DFT8_BODY(ADDROT_F, SUBROT_F, SWP " neg_lo:[1,0] neg_hi:[1,1]", FSWP " neg_hi:[1,0,0]", FSWP " neg_lo:[1,0,0]")
        : "+v"(v[0]), "+v"(v[1]), "+v"(v[2]), "+v"(v[3]), "+v"(v[4]), "+v"(v[5]), "+v"(v[6]), "+v"(v[7]),
          "=&v"(t0), "=&v"(t1), "=&v"(t2), "=&v"(t3), "=&v"(t4), "=&v"(t5), "=&v"(t6), "=&v"(t7) : "s"(R));
  else
    asm(DFT8_BODY(ADDROT_I, SUBROT_I, SWP " neg_lo:[1,1] neg_hi:[1,0]", FSWP " neg_lo:[1,0,0]", FSWP " neg_hi:[1,0,0]")
        : "+v"(v[0]), "+v"(v[1]), "+v"(v[2]), "+v"(v[3]), "+v"(v[4]), "+v"(v[5]), "+v"(v[6]), "+v"(v[7]),
          "=&v"(t0), "=&v"(t1), "=&v"(t2), "=&v"(t3), "=&v"(t4), "=&v"(t5), "=&v"(t6), "=&v"(t7) : "s"(R));
  v[0] = t4; v[1] = t2; v[2] = t6; v[3] = t3; v[4] = t5; v[5] = t0; v[6] = t7; v[7] = t1;
}
#define DFT4X2_BODY(AR, SR)                                                                                        \
  "v_pk_add_f32 %8, %0, %2\n\tv_pk_add_f32 %9, %0, %2 " SUBM "\n\t"                                                \
  "v_pk_add_f32 %10, %1, %3\n\tv_pk_add_f32 %11, %1, %3 " SUBM "\n\t"                                              \
  "v_pk_add_f32 %12, %4, %6\n\tv_pk_add_f32 %13, %4, %6 " SUBM "\n\t"                                              \
  "v_pk_add_f32 %14, %5, %7\n\tv_pk_add_f32 %15, %5, %7 " SUBM "\n\t"                                              \
  "v_pk_add_f32 %0, %8, %10\n\tv_pk_add_f32 %2, %8, %10 " SUBM "\n\t"                                              \
  "v_pk_add_f32 %1, %9, %11 " AR "\n\tv_pk_add_f32 %3, %9, %11 " SR "\n\t"                                         \
  "v_pk_add_f32 %4, %12, %14\n\tv_pk_add_f32 %6, %12, %14 " SUBM "\n\t"                                            \
  "v_pk_add_f32 %5, %13, %15 " AR "\n\tv_pk_add_f32 %7, %13, %15 " SR
template <int INV> DI void dft4x2(cf (&v)[8]) {
  cf t0, t1, t2, t3, t4, t5, t6, t7;
  if (!INV)
    asm(DFT4X2_BODY(ADDROT_F, SUBROT_F)
        : "+v"(v[0]), "+v"(v[1]), "+v"(v[2]), "+v"(v[3]), "+v"(v[4]), "+v"(v[5]), "+v"(v[6]), "+v"(v[7]),
          "=&v"(t0), "=&v"(t1), "=&v"(t2), "=&v"(t3), "=&v"(t4), "=&v"(t5), "=&v"(t6), "=&v"(t7));
  else
    asm(DFT4X2_BODY(ADDROT_I, SUBROT_I)
        : "+v"(v[0]), "+v"(v[1]), "+v"(v[2]), "+v"(v[3]), "+v"(v[4]), "+v"(v[5]), "+v"(v[6]), "+v"(v[7]),
          "=&v"(t0), "=&v"(t1), "=&v"(t2), "=&v"(t3), "=&v"(t4), "=&v"(t5), "=&v"(t6), "=&v"(t7));
}
#define TWM(t, a, w) "v_pk_mul_f32 %" #t ", %" #a ", %" #w " op_sel_hi:[1,0]\n\t"
#define TWF(t, a, w, N) "v_pk_fma_f32 %" #t ", %" #a ", %" #w ", %" #t " op_sel:[1,1,0] op_sel_hi:[0,1,1] " N "\n\t"
#define TW7_BODY(N)                                                                                                \
  "s_nop 0\n\t" TWM(0, 7, 14) TWM(1, 8, 15) TWM(2, 9, 16) TWM(3, 10, 17) TWM(4, 11, 18) TWM(5, 12, 19) TWM(6, 13, 20) \
  TWF(0, 7, 14, N) TWF(1, 8, 15, N) TWF(2, 9, 16, N) TWF(3, 10, 17, N) TWF(4, 11, 18, N) TWF(5, 12, 19, N) TWF(6, 13, 20, N)
template <int INV> DI void tw7(cf (&v)[8], const cf (&w)[7]) {
  cf t0, t1, t2, t3, t4, t5, t6;
  if (!INV)
    asm(TW7_BODY("neg_lo:[0,1,0]")
        : "=&v"(t0), "=&v"(t1), "=&v"(t2), "=&v"(t3), "=&v"(t4), "=&v"(t5), "=&v"(t6)
        : "v"(v[1]), "v"(v[2]), "v"(v[3]), "v"(v[4]), "v"(v[5]), "v"(v[6]), "v"(v[7]),
          "v"(w[0]), "v"(w[1]), "v"(w[2]), "v"(w[3]), "v"(w[4]), "v"(w[5]), "v"(w[6]));
  else
    asm(TW7_BODY("neg_hi:[0,1,0]")
        : "=&v"(t0), "=&v"(t1), "=&v"(t2), "=&v"(t3), "=&v"(t4), "=&v"(t5), "=&v"(t6)
        : "v"(v[1]), "v"(v[2]), "v"(v[3]), "v"(v[4]), "v"(v[5]), "v"(v[6]), "v"(v[7]),
          "v"(w[0]), "v"(w[1]), "v"(w[2]), "v"(w[3]), "v"(w[4]), "v"(w[5]), "v"(w[6]));
  v[1] = t0; v[2] = t1; v[3] = t2; v[4] = t3; v[5] = t4; v[6] = t5; v[7] = t6;
}
#define FL1(r, p, a) "v_pk_mul_f32 %" #r ", %" #p ", %" #a " op_sel_hi:[1,0]\n\t"
#define FL2(r, p, a) "v_pk_fma_f32 %" #r ", %" #p ", %" #a ", %" #r " op_sel:[1,1,0] op_sel_hi:[0,1,1] neg_lo:[0,1,0]\n\t"
#define FL3(r, b, q) "v_pk_fma_f32 %" #r ", %" #b ", %" #q ", %" #r " op_sel_hi:[1,0,1]\n\t"
#define FL4(r, b, q) "v_pk_fma_f32 %" #r ", %" #b ", %" #q ", %" #r " op_sel:[1,1,0] op_sel_hi:[0,1,1] neg_hi:[0,1,0]\n\t"
DI void filt4(cf* v, const cf* q, const cf* al, const cf* be) {
  cf r0, r1, r2, r3;
  asm(FL1(0, 4, 12) FL1(1, 5, 13) FL1(2, 6, 14) FL1(3, 7, 15) FL2(0, 4, 12) FL2(1, 5, 13) FL2(2, 6, 14) FL2(3, 7, 15)
      FL3(0, 16, 8) FL3(1, 17, 9) FL3(2, 18, 10) FL3(3, 19, 11) FL4(0, 16, 8) FL4(1, 17, 9) FL4(2, 18, 10) FL4(3, 19, 11)
      : "=&v"(r0), "=&v"(r1), "=&v"(r2), "=&v"(r3)
      : "v"(v[0]), "v"(v[1]), "v"(v[2]), "v"(v[3]), "v"(q[0]), "v"(q[1]), "v"(q[2]), "v"(q[3]),
        "v"(al[0]), "v"(al[1]), "v"(al[2]), "v"(al[3]), "v"(be[0]), "v"(be[1]), "v"(be[2]), "v"(be[3]));
  v[0] = r0; v[1] = r1; v[2] = r2; v[3] = r3;
}
DI void sbf(cf a, cf b, cf& p, cf& m) {
  float px, py, mx, my;
  asm("v_add_f32 %0, %4, %6\n\tv_add_f32 %1, %5, %7\n\tv_sub_f32 %2, %4, %6\n\tv_sub_f32 %3, %5, %7"
      : "=&v"(px), "=&v"(py), "=&v"(mx), "=&v"(my) : "v"(a.x), "v"(a.y), "v"(b.x), "v"(b.y));
  p = cf{px, py}; m = cf{mx, my};
}
template <int INV> DI void sbr(cf a, cf t, cf& p, cf& m) {
  float ux, uy, wx, wy;
  asm("v_add_f32 %0, %4, %7\n\tv_sub_f32 %1, %5, %6\n\tv_sub_f32 %2, %4, %7\n\tv_add_f32 %3, %5, %6"
      : "=&v"(ux), "=&v"(uy), "=&v"(wx), "=&v"(wy) : "v"(a.x), "v"(a.y), "v"(t.x), "v"(t.y));
  if (INV) { p = cf{wx, wy}; m = cf{ux, uy}; } else { p = cf{ux, uy}; m = cf{wx, wy}; }
}
DI void sbk(cf a, cf b, float k, cf& p, cf& m) {
  float px, py, mx, my;
  asm("v_fma_f32 %0, %6, %8, %4\n\tv_fma_f32 %1, %7, %8, %5\n\tv_fma_f32 %2, -%6, %8, %4\n\tv_fma_f32 %3, -%7, %8, %5"
      : "=&v"(px), "=&v"(py), "=&v"(mx), "=&v"(my) : "v"(a.x), "v"(a.y), "v"(b.x), "v"(b.y), "s"(k));
  p = cf{px, py}; m = cf{mx, my};
}
template <int INV> DI void sbkr(cf a, cf t, float k, cf& p, cf& m) {
  float ux, uy, wx, wy;
  asm("v_fma_f32 %0, %7, %8, %4\n\tv_fma_f32 %1, -%6, %8, %5\n\tv_fma_f32 %2, -%7, %8, %4\n\tv_fma_f32 %3, %6, %8, %5"
      : "=&v"(ux), "=&v"(uy), "=&v"(wx), "=&v"(wy) : "v"(a.x), "v"(a.y), "v"(t.x), "v"(t.y), "s"(k));
  if (INV) { p = cf{wx, wy}; m = cf{ux, uy}; } else { p = cf{ux, uy}; m = cf{wx, wy}; }
}
template <int INV> DI cf scm(cf a, cf w) {
  float rx, ry;
  if (!INV) asm("v_mul_f32 %0, %2, %4\n\tv_mul_f32 %1, %3, %4\n\tv_fma_f32 %0, -%3, %5, %0\n\tv_fma_f32 %1, %2, %5, %1"
                : "=&v"(rx), "=&v"(ry) : "v"(a.x), "v"(a.y), "v"(w.x), "v"(w.y));
  else asm("v_mul_f32 %0, %2, %4\n\tv_mul_f32 %1, %3, %4\n\tv_fma_f32 %0, %3, %5, %0\n\tv_fma_f32 %1, -%2, %5, %1"
           : "=&v"(rx), "=&v"(ry) : "v"(a.x), "v"(a.y), "v"(w.x), "v"(w.y));
  return cf{rx, ry};
}
template <int INV> DI void sdft4(cf& y0, cf& y1, cf& y2, cf& y3) {
  cf c0, d0, c1, e;
  sbf(y0, y2, c0, d0); sbf(y1, y3, c1, e);
  sbf(c0, c1, y0, y2); sbr<INV>(d0, e, y1, y3);
}
template <int INV> DI void sdft8(cf (&v)[8]) {
  const float R = 0.70710678118654752f;
  cf t0, t1, t2, t3, t4, t5, t6, t7;
  sbf(v[0], v[4], t0, t4); sbf(v[1], v[5], t1, t5); sbf(v[2], v[6], t2, t6); sbf(v[3], v[7], t3, t7);
  cf ps, ms, sg, dd, j0, j1, c0, d0;
  sbf(t5, t7, ps, ms);
  sbr<INV>(t4, t6, c0, d0);
  {
    float sx, sy, dx, dy;
    if (!INV) asm("v_add_f32 %0, %6, %5\n\tv_sub_f32 %1, %7, %4\n\tv_add_f32 %2, %4, %7\n\tv_sub_f32 %3, %5, %6"
                  : "=&v"(sx), "=&v"(sy), "=&v"(dx), "=&v"(dy) : "v"(ps.x), "v"(ps.y), "v"(ms.x), "v"(ms.y));
    else asm("v_sub_f32 %0, %6, %5\n\tv_add_f32 %1, %7, %4\n\tv_sub_f32 %2, %4, %7\n\tv_add_f32 %3, %5, %6"
             : "=&v"(sx), "=&v"(sy), "=&v"(dx), "=&v"(dy) : "v"(ps.x), "v"(ps.y), "v"(ms.x), "v"(ms.y));
    sg = cf{sx, sy}; dd = cf{dx, dy};
  }
  (void)j0; (void)j1;
  sdft4<INV>(t0, t1, t2, t3);
  v[0] = t0; v[2] = t1; v[4] = t2; v[6] = t3;
  sbk(c0, sg, R, v[1], v[5]);
  sbkr<INV>(d0, dd, R, v[3], v[7]);
}
DI cf sfilt(cf p, cf q, cf a, cf b) {
  float rx, ry;
  asm("v_mul_f32 %0, %2, %6\n\tv_mul_f32 %1, %3, %6\n\tv_fma_f32 %0, -%3, %7, %0\n\tv_fma_f32 %1, %2, %7, %1\n\t"
      "v_fma_f32 %0, %8, %4, %0\n\tv_fma_f32 %1, %9, %4, %1\n\tv_fma_f32 %0, %9, %5, %0\n\tv_fma_f32 %1, -%8, %5, %1"
      : "=&v"(rx), "=&v"(ry) : "v"(p.x), "v"(p.y), "v"(q.x), "v"(q.y), "v"(a.x), "v"(a.y), "v"(b.x), "v"(b.y));
  return cf{rx, ry};
}
template <int INV, int SC> DI void xdft8(cf (&v)[8]) { if (SC) sdft8<INV>(v); else dft8<INV>(v); }
template <int INV, int SC> DI void xdft4x2(cf (&v)[8]) { if (SC) { sdft4<INV>(v[0], v[1], v[2], v[3]); sdft4<INV>(v[4], v[5], v[6], v[7]); } else dft4x2<INV>(v); }
template <int INV, int SC> DI void xtw7(cf (&v)[8], const cf (&w)[7]) {
  if (SC) {
#pragma unroll
    for (int i = 1; i < 8; ++i) v[i] = scm<INV>(v[i], w[i - 1]);
  } else tw7<INV>(v, w);
}
#define CNDD(d, s0, s1, QP) "v_cndmask_b32_dpp %" #d ", %" #s0 ", %" #s1 ", vcc quad_perm:" QP " row_mask:0xf bank_mask:0xf\n\t"
#define SW4_BODY(QP)                                                                                               \
  "s_mov_b64 vcc, %16\n\ts_nop 0\n\t" CNDD(4, 8, 12, QP) CNDD(5, 9, 13, QP) CNDD(6, 10, 14, QP) CNDD(7, 11, 15, QP)   \
  "s_not_b64 vcc, vcc\n\t" CNDD(0, 12, 8, QP) CNDD(1, 13, 9, QP) CNDD(2, 14, 10, QP) CNDD(3, 15, 11, QP)
template <int X> DI void sw4(float& a0, float& a1, float& a2, float& a3, float& b0, float& b1, float& b2, float& b3) {
  float c0, c1, c2, c3, d0, d1, d2, d3;
  if (X == 1) {
    const u64 m = 0xAAAAAAAAAAAAAAAAull;
    asm(SW4_BODY("[1,0,3,2]") : "=&v"(c0), "=&v"(c1), "=&v"(c2), "=&v"(c3), "=&v"(d0), "=&v"(d1), "=&v"(d2), "=&v"(d3)
        : "v"(a0), "v"(a1), "v"(a2), "v"(a3), "v"(b0), "v"(b1), "v"(b2), "v"(b3), "s"(m) : "vcc", "scc");
  } else {
    const u64 m = 0xCCCCCCCCCCCCCCCCull;
    asm(SW4_BODY("[2,3,0,1]") : "=&v"(c0), "=&v"(c1), "=&v"(c2), "=&v"(c3), "=&v"(d0), "=&v"(d1), "=&v"(d2), "=&v"(d3)
        : "v"(a0), "v"(a1), "v"(a2), "v"(a3), "v"(b0), "v"(b1), "v"(b2), "v"(b3), "s"(m) : "vcc", "scc");
  }
  a0 = c0; a1 = c1; a2 = c2; a3 = c3; b0 = d0; b1 = d1; b2 = d2; b3 = d3;
}
template <int X> DI void swc(cf& a0, cf& a1, cf& b0, cf& b1) {
  float p0 = a0.x, p1 = a0.y, p2 = a1.x, p3 = a1.y, q0 = b0.x, q1 = b0.y, q2 = b1.x, q3 = b1.y;
  sw4<X>(p0, p1, p2, p3, q0, q1, q2, q3);
  a0 = cf{p0, p1}; a1 = cf{p2, p3}; b0 = cf{q0, q1}; b1 = cf{q2, q3};
}
DI void quadT2(cf (&v)[8]) {
  swc<1>(v[0], v[2], v[1], v[3]); swc<1>(v[4], v[6], v[5], v[7]);
  swc<2>(v[0], v[1], v[2], v[3]); swc<2>(v[4], v[5], v[6], v[7]);
}
template <int CTRL> DI float dppf(float v) { return __int_as_float(__builtin_amdgcn_update_dpp(0, __float_as_int(v), CTRL, 0xf, 0xf, true)); }
template <int CTRL> DI unsigned dppu(unsigned v) { return (unsigned)__builtin_amdgcn_update_dpp(0, (int)v, CTRL, 0xf, 0xf, true); }
DI float red32(float v) {
  v += dppf<0xB1>(v); v += dppf<0x4E>(v); v += dppf<0x141>(v); v += dppf<0x140>(v);
  auto r = __builtin_amdgcn_permlane16_swap(__float_as_uint(v), __float_as_uint(v), false, false);
  return __uint_as_float(r[0]) + __uint_as_float(r[1]);
}
DI float sadd(float a, float b) { float r; asm("v_add_f32 %0, %1, %2" : "=v"(r) : "v"(a), "v"(b)); return r; }
DI int opaque(int v) { asm volatile("" : "+v"(v)); return v; }

DI LAS char* ldsp(unsigned off) { return (LAS char*)(uintptr_t)off; }
DI cf lds_ld(unsigned off) { return *(LAS cf*)ldsp(off); }
DI void lds_st(unsigned off, cf v) { *(LAS cf*)ldsp(off) = v; }
DI void wait_lgkm() { asm volatile("s_waitcnt lgkmcnt(0)" ::: "memory"); }
DI void fence_c() { asm volatile("" ::: "memory"); }
template <int N> DI void wait_vm() { asm volatile("s_waitcnt vmcnt(%0)" ::"i"(N) : "memory"); }
DI void ctl_add(int idx) {
  const unsigned a = L_CTL + 4 * idx, one = 1u;
  asm volatile("s_waitcnt lgkmcnt(0)\n\ts_mov_b64 exec, 1\n\tds_add_u32 %0, %1\n\ts_mov_b64 exec, -1" ::"v"(a), "v"(one) : "memory");
}
DI void ctl_add_nw(int idx) {
  const unsigned a = L_CTL + 4 * idx, one = 1u;
  asm volatile("s_mov_b64 exec, 1\n\tds_add_u32 %0, %1\n\ts_mov_b64 exec, -1" ::"v"(a), "v"(one) : "memory");
}
DI unsigned ctl_add_rtn(int idx) {
  const unsigned a = L_CTL + 4 * idx, one = 1u;
  unsigned r;
  asm volatile("s_waitcnt lgkmcnt(0)\n\ts_mov_b64 exec, 1\n\tds_add_rtn_u32 %0, %1, %2\n\ts_mov_b64 exec, -1\n\ts_waitcnt lgkmcnt(0)"
               : "=&v"(r) : "v"(a), "v"(one) : "memory");
  return __builtin_amdgcn_readfirstlane(r);
}
DI void ctl_set(int idx, unsigned v) {
  const unsigned a = L_CTL + 4 * idx;
  asm volatile("s_waitcnt lgkmcnt(0)\n\ts_mov_b64 exec, 1\n\tds_write_b32 %0, %1\n\ts_mov_b64 exec, -1" ::"v"(a), "v"(v) : "memory");
}
DI unsigned ctl_get(int idx) { return __builtin_amdgcn_readfirstlane(*(volatile LAS unsigned*)ldsp(L_CTL + 4 * idx)); }
template <int SLEEP> DI void ctl_wait(int idx, unsigned target) {
  const unsigned a = L_CTL + 4 * idx;
  unsigned v, sv, cnt;
  asm volatile("s_mov_b32 %2, 0x80000\n"
               "1:\n\tds_read_b32 %0, %3\n\ts_waitcnt lgkmcnt(0)\n\tv_readfirstlane_b32 %1, %0\n\ts_cmp_ge_u32 %1, %4\n\ts_cbranch_scc1 2f\n\t"
               "s_sleep %5\n\ts_sub_u32 %2, %2, 1\n\ts_cbranch_scc0 1b\n2:"
               : "=&v"(v), "=&s"(sv), "=&s"(cnt) : "v"(a), "s"(__builtin_amdgcn_readfirstlane(target)), "i"(SLEEP) : "memory", "scc");
}
template <int SLEEP> DI void full_wait(unsigned target, int pair) {
  const unsigned a = L_CTL + 8u * pair, b = L_CTL + 4u * C_PAUSE;
  unsigned v0, v1, v2, sv, cnt;
  asm volatile("s_mov_b32 %4, 0x80000\n"
               "1:\n\tds_read_b32 %0, %5\n\tds_read_b32 %1, %5 offset:4\n\tds_read_b32 %2, %8\n\ts_waitcnt lgkmcnt(0)\n\t"
               "v_min3_u32 %0, %0, %1, %2\n\ts_nop 0\n\tv_readfirstlane_b32 %3, %0\n\ts_cmp_ge_u32 %3, %6\n\ts_cbranch_scc1 2f\n\t"
               "s_sleep %7\n\ts_sub_u32 %4, %4, 1\n\ts_cbranch_scc0 1b\n2:"
               : "=&v"(v0), "=&v"(v1), "=&v"(v2), "=&s"(sv), "=&s"(cnt)
               : "v"(a), "s"(__builtin_amdgcn_readfirstlane(target)), "i"(SLEEP), "v"(b) : "memory", "scc");
}
DI void fft_end(unsigned target) { if (ctl_add_rtn(C_FEND) + 1 == target) ctl_set(C_PAUSE, ~0u); }
DI void glds(unsigned voff, const void* sbase, unsigned ldsdst) {
  unsigned keep;
  asm volatile("s_mov_b32 %0, m0\n\ts_mov_b32 m0, %3\n\ts_nop 0\n\tglobal_load_lds_dwordx4 %1, %2\n\ts_mov_b32 m0, %0"
               : "=&s"(keep) : "v"(voff), "s"(sbase), "s"(ldsdst) : "memory");
}
DI void glds_m(unsigned voff, const void* sbase, unsigned ldsdst, u64 mask) {
  unsigned keep;
  asm volatile("s_mov_b32 %0, m0\n\ts_mov_b32 m0, %3\n\ts_mov_b64 exec, %4\n\tglobal_load_lds_dwordx4 %1, %2\n\ts_mov_b64 exec, -1\n\ts_mov_b32 m0, %0"
               : "=&s"(keep) : "v"(voff), "s"(sbase), "s"(ldsdst), "s"(mask) : "memory");
}
typedef __amdgpu_buffer_rsrc_t rsrc_t;
DI u32x4 ld16(rsrc_t r, unsigned off) { return __builtin_amdgcn_raw_buffer_load_b128(r, off, 0, 16); }
DI u32x2 ld8(rsrc_t r, unsigned off) { return __builtin_amdgcn_raw_buffer_load_b64(r, off, 0, 16); }
DI unsigned ld4(rsrc_t r, unsigned off) { return __builtin_amdgcn_raw_buffer_load_b32(r, off, 0, 16); }
DI void st16(rsrc_t r, unsigned off, u32x4 v) { __builtin_amdgcn_raw_buffer_store_b128(v, r, off, 0, 16); }
DI void st8(rsrc_t r, unsigned off, u32x2 v) { __builtin_amdgcn_raw_buffer_store_b64(v, r, off, 0, 16); }
DI void st4(rsrc_t r, unsigned off, unsigned v) { __builtin_amdgcn_raw_buffer_store_b32(v, r, off, 0, 16); }
DI u32x4 cc2(cf a, cf b) { return u32x4{__float_as_uint(a.x), __float_as_uint(a.y), __float_as_uint(b.x), __float_as_uint(b.y)}; }
DI cf u2c(unsigned a, unsigned b) { return cf{__uint_as_float(a), __uint_as_float(b)}; }

DI u32x2 make_tag(u32x2 epoch) {
  u64 z = (((u64)__builtin_amdgcn_readfirstlane(epoch.y) << 32) | (unsigned)__builtin_amdgcn_readfirstlane(epoch.x)) + 0x9E3779B97F4A7C15ull;
  z = (z ^ (z >> 30)) * 0xBF58476D1CE4E5B9ull; z = (z ^ (z >> 27)) * 0x94D049BB133111EBull; z ^= z >> 31;
  return u32x2{(unsigned)z, (unsigned)(z >> 32)};
}
template <int NREP> DI void publish(rsrc_t ws, int cidx, unsigned target, unsigned flag_off, u32x2 tag, int lane) {
  wait_vm<0>();
  const unsigned r = ctl_add_rtn(cidx);
  if (r + 1 == target && lane < NREP) st8(ws, flag_off + 2048u * lane, tag);
}
DI void publish_cnt(const Args& A, rsrc_t ws, int cidx, unsigned target, int ph, int phase, u32x2 tag, int lane) {
  wait_vm<0>();
  if (ctl_add_rtn(cidx) + 1 == target) {
    unsigned* c = (unsigned*)(A.ws + WS_CNT + 2048u * ph);
    unsigned o = 0;
    if (lane == 0) o = __hip_atomic_fetch_add(c + 32 * (blockIdx.x & 7), 1u, __ATOMIC_RELAXED, __HIP_MEMORY_SCOPE_AGENT);
    if (__builtin_amdgcn_readfirstlane(o) == 31u) {
      if (lane == 0) o = __hip_atomic_fetch_add(c + 256, 1u, __ATOMIC_RELAXED, __HIP_MEMORY_SCOPE_AGENT);
      if (__builtin_amdgcn_readfirstlane(o) == 7u && lane < 8) st8(ws, WS_GO + 1024u * phase + 128u * lane, tag);
    }
  }
}
template <int SL> DI void poll_flags(rsrc_t ws, unsigned flag_off, int nflags, u32x2 tag, int lane) {
  const unsigned o = flag_off + 32u * lane;
  bool done = 4 * lane >= nflags;
  for (unsigned spins = 0;; ++spins) {
    fence_c();
    if (!done) {
      const u32x4 a = ld16(ws, o), b = ld16(ws, o + 16);
      done = a.x == tag.x && a.y == tag.y && a.z == tag.x && a.w == tag.y && b.x == tag.x && b.y == tag.y && b.z == tag.x && b.w == tag.y;
    }
    if (__all(done)) break;
    if (spins > SPIN_CAP) break;
    __builtin_amdgcn_s_sleep(SL);
  }
  fence_c();
}
DI void wait_all(rsrc_t ws, unsigned flags, int phase, u32x2 tag, int lane, int nflags = 256, bool sweep = true) {
  const int bid = blockIdx.x;
  if (bid == AGG && sweep) {
    poll_flags<0>(ws, flags, nflags, tag, lane);
    if (lane < 8) st8(ws, WS_GO + 1024u * phase + 128u * lane, tag);
  } else {
    const unsigned go = WS_GO + 1024u * phase + 128u * (bid & 7);
    for (unsigned spins = 0; spins < 4 * SPIN_CAP; ++spins) {
      fence_c();
      const u32x2 a = ld8(ws, go);
      if (a.x == tag.x && a.y == tag.y) break;
      __builtin_amdgcn_s_sleep(GSLEEP);
    }
    fence_c();
  }
}
DI void twiddles(cf (&w)[7], int pos, float scale) {
#pragma unroll
  for (int i = 1; i < 8; ++i) { w[i - 1] = cis(-(float)(pos * i) * scale); asm volatile("" : "+v"(w[i - 1])); }
}

struct Tile { int mt, nt, np, start, mt2, nt2, rot1, rot2; };
DI Tile tile_of(int bid) {
  Tile T;
  T.mt = (bid & 1) * 8 + ((bid >> 3) & 7); T.nt = ((bid >> 1) & 3) * 4 + (bid >> 6);
  T.np = ((T.nt & 3) == 3 || T.nt == 14) ? 2 : 3;
  T.start = 3 * T.nt - (T.nt >> 2) - (T.nt == 15 ? 1 : 0);
  T.mt2 = (bid & 7) * 4 + ((bid >> 3) & 3); T.nt2 = bid >> 5;
  T.rot1 = 0;
  T.rot2 = (11 * T.nt2 + 22 * (T.mt2 & 3)) % 86;
  return T;
}
struct __attribute__((packed, aligned(2))) U8 { unsigned a, b; };
DI float smul(float a, float b) { float r; asm("v_mul_f32 %0, %1, %2" : "=v"(r) : "v"(a), "v"(b)); return r; }
DI float ssub(float a, float b) { float r; asm("v_sub_f32 %0, %1, %2" : "=v"(r) : "v"(a), "v"(b)); return r; }
DI float sfma(float a, float b, float c) { float r; asm("v_fma_f32 %0, %1, %2, %3" : "=v"(r) : "v"(a), "v"(b), "v"(c)); return r; }
DI void lane_swap(float& a, float& b, int lane) {
  const int sa = __builtin_amdgcn_readlane(__float_as_int(a), 59), sb = __builtin_amdgcn_readlane(__float_as_int(b), 63);
  a = lane == 59 ? __int_as_float(sb) : a;
  b = lane == 63 ? __int_as_float(sa) : b;
}
DI void special_swap(cf (&v)[8], int lane) {
#pragma unroll
  for (int r = 1; r < 4; ++r) {
    float ax = v[8 - r].x, ay = v[8 - r].y, bx = v[r].x, by = v[r].y;
    lane_swap(ax, bx, lane); lane_swap(ay, by, lane);
    v[8 - r] = cf{ax, ay}; v[r] = cf{bx, by};
  }
}
DI void twiddles_c(cf (&w)[7], int b, bool side) {
#pragma unroll
  for (int i = 1; i < 8; ++i) { w[i - 1] = cis(-(float)(b * (side ? 8 - i : i)) * (1.0f / 2048.0f)); asm volatile("" : "+v"(w[i - 1])); }
}

template <int mode> DI void fft_role(const Args& A, const int t_, rsrc_t ws, u32x2 epoch) {
  u32x2 tag = {0u, 0u};
  const int t = t_;
  const int lane = t & 63, w = __builtin_amdgcn_readfirstlane(t >> 6), bid = blockIdx.x;
  constexpr bool WAIT = mode & PH_WAIT;
  const int hf = lane >> 5;
  const int l5 = lane & 31, k1l = (lane >> 2) & 7, llo = lane & 3, kb = k1l + 8 * llo;
  unsigned fs = 0;
  auto fsync = [&]() { fs += 4; ctl_add(C_FSYNC); ctl_wait<0>(C_FSYNC, fs); };
  const int k0 = 4 * (t >> 3) + 1 + (t & 3);
  const int kt = (t & 4) ? (t == 255 ? 0 : 256 - k0) : k0;
  cf tw2[7];
  cf v[8];
  auto gather_cs = [&](int ln) {
    const Tile T = tile_of(bid);
    const unsigned co = WS_CS + 4u * ((w - 2) * 1408 + 32 * T.start + 2 * (ln < 48 ? ln : 0));
    u32x2 cv[8];
#pragma unroll
    for (int z = 0; z < 8; ++z) cv[z] = ld8(ws, co + 11264u * z);
    float a0 = 0.f, a1 = 0.f;
#pragma unroll
    for (int z = 0; z < 8; ++z) { a0 = sadd(a0, __uint_as_float(cv[z].x)); a1 = sadd(a1, __uint_as_float(cv[z].y)); }
    if (ln < 48) lds_st(L_CS + 384u * (w - 2) + 8u * ln, cf{a0, a1});
    ctl_add(C_CSR);
  };
  auto stat_ld = [&](int tt) { const int n1 = tile_of(bid).mt * 128 + (tt - 128); return ld8(ws, WS_STATS + 64u * (n1 & 255) + 8u * (n1 >> 8)); };
  if ((mode & PH_G1) && !(mode & PH_P2) && w >= 2) {
    gather_cs(lane);
    *(LAS u32x2*)ldsp(L_STAT + 8u * (t - 128)) = stat_ld(t); ctl_add(C_MM);
  }
  if (mode & PH_P0) {
    const int ra = hf ? (w ? 8 - w : 4) : w, rn = (8 - ra) & 7;
    unsigned raw[8];
    const unsigned* xr = (const unsigned*)(A.x + (size_t)(bid + 256 * ra) * D_);
#pragma unroll
    for (int i = 0; i < 8; ++i) raw[i] = xr[l5 + 32 * i];
    fence_c(); __builtin_amdgcn_s_barrier(); fence_c();
    if (WAIT && bid == AGG && t < 32) st4(ws, WS_CNT + 128u * t, 0u);
    cf twA[7], twc[7];
    twiddles(twA, l5, 1.0f / 256.0f);
    twiddles(tw2, llo, 1.0f / 32.0f);
    twiddles_c(twc, bid, t & 4);
    const unsigned aW1 = L_FFT + 8u * (ra * EP + l5), aR1 = L_FFT + 8u * (ra * EP + k1l * KP + llo);
    const unsigned aLo = L_FFT + 8u * (ra * EP + kb), aHi = L_FFT + 8u * (rn * EP + kb);
    const unsigned a0 = kb ? aLo : aHi, a4 = kb ? aHi : aLo;
    __builtin_amdgcn_sched_barrier(0);
    cf s2 = {0.f, 0.f};
#pragma unroll
    for (int i = 0; i < 8; ++i) { v[i] = cf{bflo(raw[i]), bfhi(raw[i])}; s2 += v[i]; }
    const float mean = red32(s2.x + s2.y) * (1.0f / D_);
    cf q2 = {0.f, 0.f};
#pragma unroll
    for (int i = 0; i < 8; ++i) { v[i] = v[i] - mean; q2 += v[i] * v[i]; }
    const float mb = rbf(mean);
    const float rinv = __builtin_amdgcn_rcpf(rbf(__builtin_amdgcn_sqrtf(rbf(rbf(red32(q2.x + q2.y) * (1.0f / D_)) + 1e-5f))));
    const float moff = (mean - mb) * rinv;
#pragma unroll
    for (int i = 0; i < 8; ++i) v[i] = v[i] * rinv + moff;
    lds_st(L_ST8 + 8u * ra, cf{mb, rinv});
    xdft8<0, SC0>(v); xtw7<0, SC0>(v, twA);
#pragma unroll
    for (int i = 0; i < 8; ++i) lds_st(aW1 + 8u * KP * i, v[i]);
    fence_c();
#pragma unroll
    for (int i = 0; i < 8; ++i) v[i] = lds_ld(aR1 + 32u * i);
    xdft8<0, SC0>(v); xtw7<0, SC0>(v, tw2);
    quadT2(v);
    xdft4x2<0, SC0>(v);
    fence_c();
#pragma unroll
    for (int i = 0; i < 8; ++i) { const int n = (i >> 2) + 2 * (i & 3); lds_st((n == 0 ? a0 : n == 4 ? a4 : n < 4 ? aLo : aHi) + 256u * n, v[i]); }
    fsync();
#pragma unroll
    for (int i = 0; i < 8; ++i) v[i] = lds_ld(L_FFT + 8u * (i * EP) + 8u * kt);
    if (t < 8) { const cf sv = lds_ld(L_ST8 + 8u * t); st8(ws, WS_STATS + 64u * bid + 8u * t, u32x2{__float_as_uint(sv.x), __float_as_uint(sv.y)}); }
    xdft8<0, SC0>(v); xtw7<0, SC0>(v, twc);
    if (w == 3) special_swap(v, lane);
    const unsigned o = WS_X + (unsigned)(t >> 3) * 131072u + (unsigned)bid * 64u + 8u * (t & 7);
#pragma unroll
    for (int i = 0; i < 8; ++i) st8(ws, o + 16384u * i, u32x2{__float_as_uint(v[i].x), __float_as_uint(v[i].y)});
    if (WAIT && !SC0) fft_end(4);
    if (WAIT) { tag = make_tag(epoch); *(volatile LAS u32x2*)ldsp(L_CTL + 128u) = tag; }
    if (WAIT) { publish<1>(ws, C_EPUB, 12, WS_FA + 8u * bid, tag, lane); ctl_add_nw(C_FPUB); }
    if (bid < 176) {
      const int cg = bid % 22, ks8 = bid / 22, cp = (cg % 11) * 64 + lane;
      const unsigned* wp = (const unsigned*)(cg >= 11 ? A.wu : A.wg) + (size_t)(ks8 * 64 + w * 16) * 688 + (cp < 688 ? cp : 0);
      unsigned cw[16];
#pragma unroll
      for (int z = 0; z < 16; ++z) cw[z] = wp[z * 688];
      float c0 = 0.f, c1 = 0.f, c2 = 0.f, c3 = 0.f;
#pragma unroll
      for (int z = 0; z < 16; z += 2) { c0 = sadd(c0, bflo(cw[z])); c1 = sadd(c1, bfhi(cw[z])); c2 = sadd(c2, bflo(cw[z + 1])); c3 = sadd(c3, bfhi(cw[z + 1])); }
      lds_st(L_CS + 1024u + 512u * w + 8u * lane, cf{sadd(c0, c2), sadd(c1, c3)});
      ctl_add(C_CSW);
      if (w == 3) {
        ctl_wait<0>(C_CSW, 4u);
        cf cs = lds_ld(L_CS + 1024u + 8u * lane);
#pragma unroll
        for (int z = 1; z < 4; ++z) { const cf b = lds_ld(L_CS + 1024u + 512u * z + 8u * lane); cs = cf{sadd(cs.x, b.x), sadd(cs.y, b.y)}; }
        st8(ws, WS_CS + 4u * (ks8 * 2816 + cg * 128 + 2 * lane), u32x2{__float_as_uint(cs.x), __float_as_uint(cs.y)});
        if (WAIT) { wait_vm<0>(); if (lane == 0) st8(ws, WS_FE + 8u * bid, tag); }
      }
    }
  } else {
    twiddles(tw2, llo, 1.0f / 32.0f);
  }
  if (mode & PH_P2) {
    const int t = opaque(t_), lane = t & 63, hf = lane >> 5;
    const int k1l = (lane >> 2) & 7, llo = lane & 3, kb = k1l + 8 * llo;
    const int c = bid & 7, q = bid >> 3, eB = t & 7, p5 = t >> 3;
    const bool spq = q == 31 && (c >= 4 || c == 0);
    const int e = w + 4 * hf;
    const bool selfp = q == 31 && w == 3 && (c & 3) == 0;
    const bool sym = q == 31 && w == 3 && (c >= 5 || ((c & 3) == 0 && hf));
    const int ep = selfp ? e : e ^ 4;
    const int kcol = sym ? 0 : (hf ? 255 - 4 * q - w : 4 * q + 1 + w);
    const int dneg = c ? 255 : 256;
    const int kbp = (dneg - kb) & 31;
    const bool spec = (dneg - kb) == 256;
    cf twB[7];
    twiddles(twB, p5, 1.0f / 256.0f);
    constexpr float SC = 0.5f / (2048.0f * 256.0f);
    const cf cs = cis((float)kcol * (1.0f / 512.0f));
    const float ca = SC - SC * cs.y, cb = SC + SC * cs.y, cg = SC * cs.x;
    const unsigned aB = L_FFT + 8u * (eB * EP + p5), aR = L_FFT + 8u * (e * EP + k1l * KP + llo), aK = L_FFT + 8u * (e * EP + kb);
    const unsigned aQ = L_FFT + 8u * (ep * EP + kbp);
    if (WAIT) {
      if (w == 0) {
        ctl_wait<1>(C_EPUB, 12u);
        wait_all(ws, WS_FA, 0, tag, lane);
        ctl_set(C_AREADY, 1u);
        if (bid == AGG && lane == 0) {
          const u64 e1 = (((u64)epoch.y << 32) | epoch.x) + 1ull;
          st8(ws, WS_EPOCH, u32x2{(unsigned)e1, (unsigned)(e1 >> 32)});
        }
      } else ctl_wait<0>(C_AREADY, 1u);
    }
    const unsigned xo = WS_X + (unsigned)bid * 16384u + 8u * t;
#pragma unroll
    for (int i = 0; i < 8; ++i) { const u32x2 r = ld8(ws, xo + 2048u * i); v[i] = u2c(r.x, r.y); }
    unsigned w1[8], wp[8], y1[8], yp[8];
    {
      const unsigned wo = WS_W + (unsigned)bid * 8192u + (unsigned)e * 1024u + 4u * kb;
      const unsigned wpo = WS_W + (unsigned)bid * 8192u + (unsigned)ep * 1024u + 4u * kbp + (spec ? 128u : 0u), wp0 = spec ? wpo - 128u : wpo + 896u;
#pragma unroll
      for (int i = 0; i < 8; ++i) { const int n = (i >> 2) + 2 * (i & 3); w1[i] = ld4(ws, wo + 128u * n); wp[i] = n ? ld4(ws, wpo + 128u * (7 - n)) : ld4(ws, wp0); y1[i] = 0u; yp[i] = 0u; }
      if (spq && w == 3) {
        const unsigned o1 = WS_W2 + (unsigned)(2 * c + hf) * 1024u + 4u * kb;
        const unsigned o2 = WS_W2 + (unsigned)(2 * c + (ep >> 2)) * 1024u + 4u * kbp + (spec ? 128u : 0u), o20 = spec ? o2 - 128u : o2 + 896u;
#pragma unroll
        for (int i = 0; i < 8; ++i) { const int n = (i >> 2) + 2 * (i & 3); y1[i] = ld4(ws, o1 + 128u * n); yp[i] = n ? ld4(ws, o2 + 128u * (7 - n)) : ld4(ws, o20); }
      }
    }
    u32x2 sv = {0u, 0u};
    if ((mode & PH_G1) && w >= 2) sv = stat_ld(t);
    cf al[8], be[8];
    {
      float W1x[8], W1y[8], W2x[8], W2y[8];
#pragma unroll
      for (int i = 0; i < 8; ++i) { W1x[i] = bflo(w1[i]); W1y[i] = bfhi(w1[i]); W2x[i] = bflo(wp[i]); W2y[i] = bfhi(wp[i]); }
      if (spq && w == 3) {
#pragma unroll
        for (int i = 0; i < 8; ++i) {
          if (sym) {
            W1x[i] = smul(sadd(W1x[i], W2x[i]), 0.5f); W1y[i] = smul(ssub(W1y[i], W2y[i]), 0.5f);
            W2x[i] = smul(sadd(bflo(y1[i]), bflo(yp[i])), 0.5f); W2y[i] = smul(ssub(bfhi(yp[i]), bfhi(y1[i])), 0.5f);
          }
        }
      }
      const float ncb = -cb, ncg = -cg;
#pragma unroll
      for (int i = 0; i < 8; ++i) {
        al[i] = cf{sfma(W1x[i], ca, smul(W2x[i], cb)), sfma(W1y[i], ca, smul(W2y[i], ncb))};
        be[i] = cf{smul(sadd(W2y[i], W1y[i]), ncg), smul(ssub(W1x[i], W2x[i]), cg)};
        asm volatile("" : "+v"(al[i]), "+v"(be[i]));
      }
    }
    if ((mode & PH_G1) && w >= 2) { *(LAS u32x2*)ldsp(L_STAT + 8u * (t - 128)) = sv; ctl_add_nw(C_MM); }
#pragma unroll
    for (int i = 0; i < 8; ++i) asm volatile("" : "+v"(v[i]));
    xdft8<0, SC2>(v); xtw7<0, SC2>(v, twB);
#pragma unroll
    for (int i = 0; i < 8; ++i) lds_st(aB + 8u * KP * i, v[i]);
    fs += 4; ctl_add(C_FSYNC);
    ctl_wait<0>(C_FSYNC, fs);
#pragma unroll
    for (int i = 0; i < 8; ++i) v[i] = lds_ld(aR + 32u * i);
    xdft8<0, SC2>(v); xtw7<0, SC2>(v, tw2);
    quadT2(v);
    xdft4x2<0, SC2>(v);
    fence_c();
#pragma unroll
    for (int i = 0; i < 8; ++i) lds_st(aK + 256u * ((i >> 2) + 2 * (i & 3)), v[i]);
    fence_c();
    cf qv[8];
    {
      const unsigned aQs = aQ + (spec ? 256u : 0u);
#pragma unroll
      for (int i = 0; i < 8; ++i) {
        const int n = (i >> 2) + 2 * (i & 3);
        if (n == 0) qv[i] = lds_ld(spec ? aQ : aQ + 256u * 7);
        else qv[i] = lds_ld(aQs + 256u * (7 - n));
      }
    }
    if (SC2) {
#pragma unroll
      for (int i = 0; i < 8; ++i) v[i] = sfilt(v[i], qv[i], al[i], be[i]);
    } else { filt4(v, qv, al, be); filt4(v + 4, qv + 4, al + 4, be + 4); }
    xdft4x2<1, SC2>(v);
    quadT2(v);
    xtw7<1, SC2>(v, tw2); xdft8<1, SC2>(v);
    fence_c();
#pragma unroll
    for (int i = 0; i < 8; ++i) lds_st(aR + 32u * i, v[i]);
    fsync();
#pragma unroll
    for (int i = 0; i < 8; ++i) v[i] = lds_ld(aB + 8u * KP * i);
    xtw7<1, SC2>(v, twB); xdft8<1, SC2>(v);
    asm volatile("" : "+v"(v[7]));
    const unsigned zo = WS_Z + (unsigned)p5 * 16384u + (unsigned)c * 2048u + (unsigned)q * 64u + 8u * eB;
#pragma unroll
    for (int i = 0; i < 8; ++i) st8(ws, zo + 524288u * i, u32x2{__float_as_uint(v[i].x), __float_as_uint(v[i].y)});
    if (WAIT) publish_cnt(A, ws, C_FPUB, 8, 0, 2, tag, lane);
  }
  if (mode & PH_P3) {
    const int t = opaque(t_), lane = t & 63, hq = lane >> 5;
    const int l5 = lane & 31, k1l = (lane >> 2) & 7, llo = lane & 3, kb = k1l + 8 * llo;
    const int k0 = 4 * (t >> 3) + 1 + (t & 3);
    const int kt = (t & 4) ? (t == 255 ? 0 : 256 - k0) : k0;
    cf twA[7], twc[7];
    twiddles(twA, l5, 1.0f / 256.0f);
    twiddles_c(twc, bid, t & 4);
    const int ra = hq ? (w ? 8 - w : 4) : w, rn = (8 - ra) & 7;
    const unsigned aW1 = L_FFT + 8u * (ra * EP + l5), aR1 = L_FFT + 8u * (ra * EP + k1l * KP + llo);
    const unsigned aLo = L_FFT + 8u * (ra * EP + kb), aHi = L_FFT + 8u * (rn * EP + kb);
    const unsigned a0 = kb ? aLo : aHi, a4 = kb ? aHi : aLo;
    if (WAIT) {
      if (w == 0) { ctl_wait<1>(C_FPUB, 8u); wait_all(ws, WS_FC, 2, tag, lane, 256, false); ctl_set(C_CREADY, 1u); }
      else ctl_wait<0>(C_CREADY, 1u);
    }
    const unsigned o = WS_Z + (unsigned)bid * 16384u + 8u * t;
#pragma unroll
    for (int i = 0; i < 8; ++i) { const u32x2 r = ld8(ws, o + 2048u * i); v[i] = u2c(r.x, r.y); }
    if (w == 3) special_swap(v, lane);
    xtw7<1, SC3>(v, twc); xdft8<1, SC3>(v);
#pragma unroll
    for (int i = 0; i < 8; ++i) lds_st(L_FFT + 8u * (i * EP) + 8u * kt, v[i]);
    fsync();
#pragma unroll
    for (int i = 0; i < 8; ++i) { const int n = (i >> 2) + 2 * (i & 3); v[i] = lds_ld((n == 0 ? a0 : n == 4 ? a4 : n < 4 ? aLo : aHi) + 256u * n); }
    xdft4x2<1, SC3>(v);
    quadT2(v);
    xtw7<1, SC3>(v, tw2); xdft8<1, SC3>(v);
    fence_c();
#pragma unroll
    for (int i = 0; i < 8; ++i) lds_st(aR1 + 32u * i, v[i]);
    fence_c();
#pragma unroll
    for (int i = 0; i < 8; ++i) v[i] = lds_ld(aW1 + 8u * KP * i);
    ctl_add(C_FDONE);
    xtw7<1, SC3>(v, twA); xdft8<1, SC3>(v);
    asm volatile("" : "+v"(v[7]));
    const unsigned so = WS_S + (unsigned)(bid + 256 * ra) * 1024u + 4u * l5;
#pragma unroll
    for (int i = 0; i < 8; ++i) st4(ws, so + 128u * i, pk2(v[i].x, v[i].y));
    if (WAIT) publish_cnt(A, ws, C_FPUB, 12, 1, 3, tag, lane);
  }
}

__host__ __device__ constexpr unsigned wd_addr0(int u) {
  return u < 8 ? L_SPARE + 2048u * u : u < 28 ? 81920u + 2048u * (u - 8) : u < 48 ? 2048u * (u - 28) : 40960u + 2048u * (u - 48);
}
__host__ __device__ constexpr unsigned wd_addr(int u) { return wd_addr0(u >= 68 ? u - 68 : u); }
DI bf16x8 tr2(unsigned a0, unsigned a1) {
  const s16x4 lo = __builtin_amdgcn_ds_read_tr16_b64_v4i16((LAS s16x4*)ldsp(a0));
  const s16x4 hi = __builtin_amdgcn_ds_read_tr16_b64_v4i16((LAS s16x4*)ldsp(a1));
  return __builtin_shufflevector(lo, hi, 0, 1, 2, 3, 4, 5, 6, 7);
}
#define MFMA(a, b, c) __builtin_amdgcn_mfma_f32_32x32x16_bf16((a), (b), (c), 0, 0, 0)
DI unsigned wfrag_off(int lane, int fg) {
  const int g4 = lane >> 4, q4 = (lane >> 2) & 3, pp = lane & 3, mat = g4 & 1;
  return 16384u + (unsigned)mat * 12288u + (unsigned)(2 * (g4 >> 1)) * 768u + (unsigned)q4 * 64u + (unsigned)(fg ^ mat) * 32u + 8u * pp;
}

template <int mode> DI void load_role(const Args& A, int L, int lane) {
  const int bid = blockIdx.x, l = L & 1, pr = L >> 1;
  constexpr bool WAIT = mode & PH_WAIT;
  const Tile T = tile_of(bid);
  const int g4 = lane >> 4, q4 = (lane >> 2) & 3, pp = lane & 3;
  auto pub = [&](unsigned n) { ctl_set(C_FULL0 + L, n); };
  if (mode & PH_G1) {
    unsigned xv[2], wv[3]; u64 wm[3];
#pragma unroll
    for (int i = 0; i < 2; ++i) { const int row = i * 8 + (lane >> 3); xv[i] = (unsigned)row * 1024u + 16u * ((lane & 7) ^ ((row >> 1) & 7)); }
#pragma unroll
    for (int i = 0; i < 3; ++i) {
      const int cell = 4 * i + g4, a = cell / 3, pair = cell - 3 * a;
      wv[i] = (unsigned)(4 * a + q4) * 2752u + (unsigned)pair * 64u + 16u * (pp ^ (2 * l));
      wm[i] = __ballot(pair < T.np);
    }
    const char* xb = (const char*)A.x + (size_t)(T.mt * 128 + l * 64) * 1024;
    const char* wb = (const char*)(l ? A.wu : A.wg) + T.start * 64;
    if (mode & PH_P0) { __builtin_amdgcn_s_sleep(LDELAY); if (pr) __builtin_amdgcn_s_sleep(LDELAY2); }
#pragma nounroll
    for (int s = pr; s < 8; s += 2) {
      if (s >= 3) ctl_wait<0>(C_FR0 + s % 3, 8u * (s / 3));
      const int sr = (s + T.rot1) & 7;
      const unsigned slot = (unsigned)(s % 3) * L_SLOT;
#pragma unroll
      for (int g = 0; g < 4; ++g) {
#pragma unroll
        for (int i = 0; i < 2; ++i) { glds(xv[i], xb + g * 16384 + sr * 128, slot + (unsigned)(l * 8 + 2 * g + i) * 1024u); wait_vm<LTHR>(); }
#pragma unroll
        for (int i = 0; i < 3; ++i) {
          glds_m(wv[i], wb + (size_t)(sr * 64 + g * 16) * 2752, slot + 16384u + (unsigned)l * 12288u + (unsigned)(3 * g + i) * 1024u, wm[i]);
          wait_vm<LTHR>();
        }
      }
      wait_vm<0>(); pub(s + 1);
      if (WAIT && bid == AGG && L == 3 && s == 3) {
        const rsrc_t ws = __builtin_amdgcn_make_buffer_rsrc(A.ws, 0, WS_END, 0x00020000);
        ctl_wait<2>(C_FPUB, 1u);
        u32x2 tag = *(volatile LAS u32x2*)ldsp(L_CTL + 128u);
        tag.x = __builtin_amdgcn_readfirstlane(tag.x); tag.y = __builtin_amdgcn_readfirstlane(tag.y);
        wait_all(ws, WS_FE, 1, tag, lane, 176);
      }
    }
      if (WAIT && pr == 1) {
        const rsrc_t ws = __builtin_amdgcn_make_buffer_rsrc(A.ws, 0, WS_END, 0x00020000);
        ctl_wait<2>(C_FPUB, 1u);
        u32x2 tag = *(volatile LAS u32x2*)ldsp(L_CTL + 128u);
        tag.x = __builtin_amdgcn_readfirstlane(tag.x); tag.y = __builtin_amdgcn_readfirstlane(tag.y);
        if (l == 0) { wait_all(ws, WS_FE, 1, tag, lane, 176, false); ctl_set(C_EREADY, 1u); } else ctl_wait<1>(C_EREADY, 1u);
        const unsigned co = WS_CS + 4u * (l * 1408 + 32 * T.start + 2 * (lane < 48 ? lane : 0));
        u32x2 cv[8];
#pragma unroll
        for (int z = 0; z < 8; ++z) cv[z] = ld8(ws, co + 11264u * z);
        float a0 = 0.f, a1 = 0.f;
#pragma unroll
        for (int z = 0; z < 8; ++z) { a0 = sadd(a0, __uint_as_float(cv[z].x)); a1 = sadd(a1, __uint_as_float(cv[z].y)); }
        if (lane < 48) lds_st(L_CS + 384u * l + 8u * lane, cf{a0, a1});
        ctl_add(C_CSR);
      }
  }
  if (!(mode & PH_G2)) return;
  if (pr == 0) {
    const unsigned wdv = (unsigned)(lane >> 3) * 1024u + 16u * ((lane & 7) ^ (((lane >> 4) & 1) << 2));
    const char* wdb = (const char*)A.wd + T.nt2 * 128 + l * 8192;
    auto wd_units = [&](int u0, int u1, unsigned dst) {
#pragma nounroll
      for (int u = u0; u < u1; ++u) { const int ur = u + T.rot2 >= 86 ? u + T.rot2 - 86 : u + T.rot2; glds(wdv, wdb + (size_t)ur * 16384, dst + 2048u * (u - u0) + 1024u * l); wait_vm<LTHR2>(); }
    };
    wd_units(0, 8, L_SPARE);
    if (mode & PH_G1) ctl_wait<1>(C_FR0 + 2, 16u);
    wd_units(8, 28, 81920u);
    if (mode & PH_G1) ctl_wait<1>(C_FR0, 24u);
    wd_units(28, 48, 0u);
    wait_vm<20>(); pub(10);
    if (mode & PH_G1) ctl_wait<1>(C_FR0 + 1, 24u);
    wd_units(48, 68, 40960u);
    wait_vm<0>(); pub(12);
    ctl_wait<1>(C_FREE2, 8u);
    wd_units(68, 76, L_SPARE);
    wd_units(76, 86, 81920u);
    wait_vm<0>(); pub(14);
  } else {
    const rsrc_t ws = __builtin_amdgcn_make_buffer_rsrc(A.ws, 0, WS_END, 0x00020000);
    u32x2 tag = {0u, 0u};
    if (WAIT) { ctl_wait<2>(C_FPUB, 1u); tag = *(volatile LAS u32x2*)ldsp(L_CTL + 128u); tag.x = __builtin_amdgcn_readfirstlane(tag.x); tag.y = __builtin_amdgcn_readfirstlane(tag.y); }
    const int tok0 = T.mt2 * 64 + l * 32 + (lane >> 3);
    u32x4 xq[4], sq[4];
#pragma unroll
    for (int i = 0; i < 4; ++i) xq[i] = *(const u32x4*)(A.x + (size_t)(tok0 + 8 * i) * D_ + T.nt2 * 64 + (lane & 7) * 8);
    if (WAIT) {
      if (l == 0) { ctl_wait<2>(C_GPUB, 8u); poll_flags<BSLEEP>(ws, WS_FB + 128u * (T.mt2 >> 1), 16, tag, lane); ctl_set(C_HREADY, 1u); }
      ctl_wait<2>(C_FPUB, 12u);
      if (l == 1) { wait_all(ws, WS_FD, 3, tag, lane, 256, false); ctl_set(C_EREADY, 2u); } else ctl_wait<1>(C_EREADY, 2u);
    }
#pragma unroll
    for (int i = 0; i < 4; ++i) sq[i] = ld16(ws, WS_S + (unsigned)(tok0 + 8 * i) * 1024u + (unsigned)T.nt2 * 128u + 16u * (lane & 7));
    if (mode & PH_P3) ctl_wait<1>(C_FDONE, 4u);
    const unsigned lo = L_FFT + 144u * (32 * l + (lane >> 3)) + 16u * (lane & 7);
#pragma unroll
    for (int i = 0; i < 4; ++i) { *(LAS u32x4*)ldsp(lo + 1152u * i) = xq[i]; *(LAS u32x4*)ldsp(lo + 9216u + 1152u * i) = sq[i]; }
    ctl_add(C_SXREADY);
  }
}

#define SB() __builtin_amdgcn_sched_barrier(0)
template <int NP> DI void g1_loop(const Args& A, f32x16 (&acc)[3], unsigned xa, unsigned wa) {
  unsigned slot = 0;
  bf16x8 xf[4], wf[4][NP];
  full_wait<1>(1, 0);
#pragma unroll
  for (int k = 0; k < 2; ++k) {
    xf[k] = *(LAS bf16x8*)ldsp(xa ^ (32u * k));
    fence_c(); SB();
#pragma unroll
    for (int p = 0; p < NP; ++p) { wf[k][p] = tr2(wa + 3072u * k + p * 256u, wa + 3072u * k + 768u + p * 256u); fence_c(); SB(); }
  }
#pragma nounroll
  for (int s = 0; s < 8; ++s) {
    const unsigned nslot = slot == 2 * L_SLOT ? 0 : slot + L_SLOT;
    const unsigned pa = L_CTL + 8u * ((s + 1) & 1);
    const unsigned pk0 = *(volatile LAS unsigned*)ldsp(pa), pk1 = *(volatile LAS unsigned*)ldsp(pa + 4);
    unsigned wsl = wa + slot, xsl = xa + slot;
#pragma unroll
    for (int ks = 0; ks < 4; ++ks) {
      if (ks == 2) {
        const unsigned need = s < 7 ? (unsigned)s + 2u : 0u;
        if (__builtin_amdgcn_readfirstlane(pk0 < pk1 ? pk0 : pk1) < need) full_wait<1>(need, (s + 1) & 1);
        const unsigned sl = s < 7 ? nslot : slot;
        wsl = wa + sl; xsl = xa + sl;
      }
      const int kn = (ks + 2) & 3;
      SB();
      acc[0] = MFMA(wf[ks][0], xf[ks], acc[0]);
      SB();
      xf[kn] = *(LAS bf16x8*)ldsp(xsl ^ (32u * kn));
      wf[kn][0] = tr2(wsl + 3072u * kn, wsl + 3072u * kn + 768u);
      SB();
      acc[1] = MFMA(wf[ks][1], xf[ks], acc[1]);
      SB();
      wf[kn][1] = tr2(wsl + 3072u * kn + 256u, wsl + 3072u * kn + 1024u);
      if (NP == 3) {
        SB();
        acc[2] = MFMA(wf[ks][NP - 1], xf[ks], acc[2]);
        SB();
        wf[kn][NP - 1] = tr2(wsl + 3072u * kn + 512u, wsl + 3072u * kn + 1280u);
      }
      if (ks == 1 && s < 7) ctl_add_nw(C_FR0 + s % 3);
      if (ks == 3 && s == 7) ctl_add_nw(C_FR0 + 1);
      SB();
    }
    slot = nslot;
  }
}
template <int mode> DI void gemm_role(const Args& A, int t, rsrc_t ws, u32x2 epoch) {
  const int lane = t & 63, w = __builtin_amdgcn_readfirstlane(t >> 6);
  constexpr bool WAIT = mode & PH_WAIT;
  const Tile T = tile_of(blockIdx.x);
  const int r = lane & 31, h = lane >> 5;
  if (mode & PH_P0) {
    const int bid = blockIdx.x, rowc = bid & 7, kbp = (bid >> 3) & 15, side = bid >> 7;
    const int rr = t & 15, q = t >> 4, c = side ? (8 - rowc) & 7 : rowc;
    const int ro = (rowc + 8 * (16 * kbp + rr)) * DF_;
    const int co = ro + (side ? 252 - 4 * q : 4 * q + 1);
    const U8 a = *(const U8*)(A.wr + co), b = *(const U8*)(A.wi + co);
    fence_c(); __builtin_amdgcn_s_barrier(); fence_c();
    unsigned wd[4] = {__builtin_amdgcn_perm(b.a, a.a, 0x05040100u), __builtin_amdgcn_perm(b.a, a.a, 0x07060302u),
                      __builtin_amdgcn_perm(b.b, a.b, 0x05040100u), __builtin_amdgcn_perm(b.b, a.b, 0x07060302u)};
    if (q == 31 && (c >= 5 || ((c & 3) == 0 && side))) {
      const unsigned z0 = (unsigned)A.wr[ro] | ((unsigned)A.wi[ro] << 16), z1 = (unsigned)A.wr[ro + 256] | ((unsigned)A.wi[ro + 256] << 16);
      if (side) wd[0] = z0; else wd[3] = z0;
      st4(ws, WS_W2 + (unsigned)(2 * c + side) * 1024u + 64u * kbp + 4u * rr, z1);
    }
    asm volatile("" :: "v"(wd[0]), "v"(wd[3]));
    const unsigned o = WS_W + (unsigned)(q * 8 + c) * 8192u + (unsigned)side * 4096u + 64u * kbp + 4u * rr;
#pragma unroll
    for (int z = 0; z < 4; ++z) st4(ws, o + 1024u * (side ? 3 - z : z), wd[z]);
  }
  if (mode & PH_G1) {
    const int tg = w & 3, fg = w >> 2;
    const int xrow = tg * 32 + r;
    const unsigned xa = (unsigned)xrow * 128u + 16u * (h ^ ((xrow >> 1) & 7));
    const unsigned wa = wfrag_off(lane, fg);
    f32x16 acc[3];
#pragma unroll
    for (int p = 0; p < 3; ++p)
#pragma unroll
      for (int i = 0; i < 16; ++i) acc[p][i] = 0.f;
    u32x2 tag = {0u, 0u};
    if (WAIT) { tag = make_tag(epoch); publish<1>(ws, C_EPUB, 12, WS_FA + 8u * blockIdx.x, tag, lane); }
    if (T.np == 3) g1_loop<3>(A, acc, xa, wa); else g1_loop<2>(A, acc, xa, wa);
    ctl_wait<1>(C_MM, 2u);
    ctl_wait<1>(C_CSR, 2u);
    const cf st = lds_ld(L_STAT + 8u * xrow);
    const float m0 = rbf(-st.x), mr = -st.x - m0, m1 = rbf(mr), m2 = mr - m1;
    const u32x4 bq = {pk2(m0, m0), pk2(m0, m1), pk2(m1, m2), 0u};
    const bf16x8 bfrag = __builtin_bit_cast(bf16x8, bq);
    constexpr float K0 = -2.0f * 0.7978845608028654f * 1.4426950408889634f, K1 = K0 * 0.044715f;
#pragma unroll
    for (int p = 0; p < 3; ++p) {
      if (p < T.np) {
        const float co = *(LAS float*)ldsp(L_CS + 384u * (r >> 4) + 4u * (32 * p + 16 * fg + (r & 15)));
        const float ct = h ? 0.f : co;
        const float c0 = rbf(ct), r1 = ct - c0, c1 = rbf(r1), c2 = r1 - c1;
        const u32x4 aq = {pk2(c0, c1), pk2(c2, c0), pk2(c1, c0), 0u};
        const f32x16 a = MFMA(__builtin_bit_cast(bf16x8, aq), bfrag, acc[p]);
        unsigned hv[4];
#pragma unroll
        for (int i = 0; i < 4; ++i) {
          const cf g = cf{a[2 * i], a[2 * i + 1]} * st.y, u = cf{a[8 + 2 * i], a[9 + 2 * i]} * st.y;
          const cf z = g * (g * g * K1 + K0);
          const cf d = cf{__builtin_amdgcn_exp2f(z.x), __builtin_amdgcn_exp2f(z.y)} + 1.0f;
          const cf hh = (g * u) * cf{__builtin_amdgcn_rcpf(d.x), __builtin_amdgcn_rcpf(d.y)};
          hv[i] = pk2(hh.x, hh.y);
        }
        const int unit = 2 * (T.start + p) + fg;
        st16(ws, WS_H + (unsigned)((T.mt * 4 + tg) * 86 + unit) * 1024u + 16u * lane, u32x4{hv[0], hv[1], hv[2], hv[3]});
      }
    }
    if (WAIT) publish<1>(ws, C_GPUB, 8, WS_FB + 8u * (T.mt * 16 + T.nt), tag, lane);
  }
  if (mode & PH_G2) {
    const int th = w & 1, kq = w >> 1;
    const int g4 = lane >> 4, q4 = (lane >> 2) & 3, pp = lane & 3;
    const int dsub = g4 & 1, hh = g4 >> 1;
    unsigned wa2[2];
#pragma unroll
    for (int dh = 0; dh < 2; ++dh)
      wa2[dh] = (unsigned)kq * 2048u + (unsigned)(4 * hh + q4) * 128u + (unsigned)(dh ^ (q4 >> 1)) * 64u + (unsigned)dsub * 32u + 8u * pp;
    const unsigned wa3[2] = {wa2[0] + 81920u, wa2[1] + 81920u};
    f32x16 acc[2];
#pragma unroll
    for (int p = 0; p < 2; ++p)
#pragma unroll
      for (int i = 0; i < 16; ++i) acc[p][i] = 0.f;
    bf16x8 wq[22][2];
#define WLD(i, dh) do { const unsigned ad = wd_addr(4 * (i)); \
      const unsigned bb = ad >= 81920u ? wa3[dh] : wa2[dh], so = ad >= 81920u ? ad - 81920u : ad; \
      wq[i][dh] = tr2(bb + so, bb + so + 1024u); } while (0)
    ctl_wait<1>(C_FULL0, 10u); ctl_wait<0>(C_FULL0 + 1, 10u);
#pragma unroll
    for (int i = 0; i < 5; ++i) { WLD(i, 0); WLD(i, 1); }
    ctl_add(C_FREE2);
#pragma unroll
    for (int i = 0; i < 5; ++i) asm volatile("" : "+v"(wq[i][0]), "+v"(wq[i][1]));
    if (WAIT) ctl_wait<1>(C_HREADY, 1u);
    const unsigned ho = WS_H + (unsigned)((2 * T.mt2 + th) * 86) * 1024u + 16u * lane;
    auto hoff = [&](int i) { const int u = 4 * i + kq + T.rot2; return (unsigned)(u >= 86 ? u - 86 : u) * 1024u; };
    auto hld = [&](int i) { return __builtin_amdgcn_raw_buffer_load_b128(ws, ho, hoff(i == 21 && kq >= 2 ? 20 : i), 16); };
    constexpr int WIN = HWIN;
    u32x4 hf[22];
#pragma unroll
    for (int i = 0; i < WIN; ++i) hf[i] = hld(i);
#pragma unroll
    for (int i = 0; i < 22; ++i) {
      if (i == 0 || i == 3) full_wait<0>(10, 0);
      if (i == 6 || i == 9 || i == 12) full_wait<0>(12, 0);
      if (i == 16 || i == 19) full_wait<0>(14, 0);
      if (i == 21 && kq >= 2) hf[i] = u32x4{0u, 0u, 0u, 0u};
      const bf16x8 hb = __builtin_bit_cast(bf16x8, hf[i]);
      SB();
      acc[0] = MFMA(wq[i][0], hb, acc[0]);
      asm volatile("" : "+v"(acc[0]));
      SB();
      if (i >= 4 && i < 21) WLD(i + 1, 0);
      if (i + WIN < 22) hf[i + WIN] = hld(i + WIN);
      SB();
      acc[1] = MFMA(wq[i][1], hb, acc[1]);
      asm volatile("" : "+v"(acc[1]));
      SB();
      if (i >= 4 && i < 21) WLD(i + 1, 1);
      if (i == 12) ctl_add_nw(C_RED);
      SB();
    }
    ctl_wait<0>(C_RED, 8u);
    auto xaddr = [&](int dst, int z) { const unsigned i = (unsigned)((th * 4 + dst) * 3 + z) * 2048u; return (i < 40960u ? i : i - 40960u + 106496u) + 16u * lane; };
#pragma unroll
    for (int dst = 0; dst < 4; ++dst) {
      if (dst != kq) {
        const unsigned ad = xaddr(dst, kq > dst ? kq - 1 : kq);
        const f32x16& a = acc[dst >> 1];
        const int o = 8 * (dst & 1);
        *(LAS f32x4*)ldsp(ad) = f32x4{a[o], a[o + 1], a[o + 2], a[o + 3]};
        *(LAS f32x4*)ldsp(ad + 1024u) = f32x4{a[o + 4], a[o + 5], a[o + 6], a[o + 7]};
      }
    }
    ctl_add(C_RED);
    f32x4 o0, o1;
    {
      const f32x16 a = kq & 2 ? acc[1] : acc[0];
      o0 = kq & 1 ? f32x4{a[8], a[9], a[10], a[11]} : f32x4{a[0], a[1], a[2], a[3]};
      o1 = kq & 1 ? f32x4{a[12], a[13], a[14], a[15]} : f32x4{a[4], a[5], a[6], a[7]};
    }
    ctl_wait<0>(C_RED, 16u);
#pragma unroll
    for (int z = 0; z < 3; ++z) { const unsigned ad = xaddr(kq, z); o0 += *(LAS f32x4*)ldsp(ad); o1 += *(LAS f32x4*)ldsp(ad + 1024u); }
    const float ov[8] = {o0.x, o0.y, o0.z, o0.w, o1.x, o1.y, o1.z, o1.w};
    float gs[8];
#pragma unroll
    for (int i = 0; i < 8; ++i) gs[i] = ov[i] * __builtin_amdgcn_rcpf(1.0f + __builtin_amdgcn_exp2f(-1.4426950408889634f * ov[i]));
    {
      const unsigned ga = 40960u + 272u * (th * 32 + r) + 4u * (16 * kq + 4 * h);
      *(LAS f32x4*)ldsp(ga) = f32x4{gs[0], gs[1], gs[2], gs[3]};
      *(LAS f32x4*)ldsp(ga + 32u) = f32x4{gs[4], gs[5], gs[6], gs[7]};
    }
    ctl_add(C_RED);
    ctl_wait<0>(C_RED, 24u);
    const int tok = t >> 3, d8 = t & 7;
    f32x4 g0 = *(LAS f32x4*)ldsp(40960u + 272u * tok + 32u * d8), g1 = *(LAS f32x4*)ldsp(40960u + 272u * tok + 32u * d8 + 16u);
    asm volatile("" : "+v"(g0), "+v"(g1));
    ctl_wait<1>(C_SXREADY, 2u);
    const unsigned la = L_FFT + 144u * tok + 16u * d8;
    const u32x4 xq = *(LAS u32x4*)ldsp(la), sq = *(LAS u32x4*)ldsp(la + 9216u);
    u32x4 res;
    res.x = pk2(bflo(sq.x) * g0.x + bflo(xq.x), bfhi(sq.x) * g0.y + bfhi(xq.x));
    res.y = pk2(bflo(sq.y) * g0.z + bflo(xq.y), bfhi(sq.y) * g0.w + bfhi(xq.y));
    res.z = pk2(bflo(sq.z) * g1.x + bflo(xq.z), bfhi(sq.z) * g1.y + bfhi(xq.z));
    res.w = pk2(bflo(sq.w) * g1.z + bflo(xq.w), bfhi(sq.w) * g1.w + bfhi(xq.w));
    *(u32x4*)(A.out + (size_t)(T.mt2 * 64 + tok) * D_ + T.nt2 * 64 + d8 * 8) = res;
  }
}

template <int MODE>
__global__ __launch_bounds__(1024, 4) void fused_kernel(Args A) {
  const int tid = threadIdx.x, wv = __builtin_amdgcn_readfirstlane(tid >> 6);
  if (wv == 4) { *(volatile LAS unsigned*)ldsp(L_CTL + 4u * (tid & 63)) = ((tid & 63) == C_PAUSE && (SC0 || !(MODE & PH_WAIT))) ? ~0u : 0u; wait_lgkm(); }
  if (!(MODE & PH_P0) || (wv >= 4 && wv < 8)) __builtin_amdgcn_s_barrier();
  if (wv >= 4 && wv < 8) {
    __builtin_amdgcn_s_setprio(2);
    load_role<MODE>(A, wv - 4, tid & 63);
    return;
  }
  const rsrc_t ws = __builtin_amdgcn_make_buffer_rsrc(A.ws, 0, WS_END, 0x00020000);
  u32x2 epoch = {0u, 0u};
  if (MODE & PH_WAIT) epoch = ld8(ws, WS_EPOCH);
  if (wv >= 8) {
    if (wv >= 12) __builtin_amdgcn_s_setprio(1);
    gemm_role<MODE>(A, tid - 512, ws, epoch);
  } else {
    __builtin_amdgcn_s_setprio(3);
    fft_role<MODE>(A, tid, ws, epoch);
  }
}

extern "C" void kernel_launch(void* const* d_in, const int* in_sizes, int n_in, void* d_out, int out_size, void* d_ws,
                              size_t ws_size, hipStream_t stream) {
  Args A;
  A.x = (const uint16_t*)d_in[0]; A.wr = (const uint16_t*)d_in[1]; A.wi = (const uint16_t*)d_in[2];
  A.wg = (const uint16_t*)d_in[4]; A.wu = (const uint16_t*)d_in[5]; A.wd = (const uint16_t*)d_in[6];
  A.out = (uint16_t*)d_out; A.ws = (char*)d_ws;
#define LAUNCH(M) do { (void)hipFuncSetAttribute((const void*)fused_kernel<M>, hipFuncAttributeMaxDynamicSharedMemorySize, L_TOTAL); \
    fused_kernel<M><<<256, 1024, L_TOTAL, stream>>>(A); } while (0)
  int dev = 0, cus = 0;
  (void)hipGetDevice(&dev);
  (void)hipDeviceGetAttribute(&cus, hipDeviceAttributeMultiprocessorCount, dev);
  if (cus >= 256) {
    LAUNCH(PH_P0 | PH_G1 | PH_P2 | PH_P3 | PH_G2 | PH_WAIT);
  } else {
    LAUNCH(PH_P0);
    LAUNCH(PH_G1);
    LAUNCH(PH_P2);
    LAUNCH(PH_P3);
    LAUNCH(PH_G2);
  }
}
```
